```python
import torch

def _softmax(x, axis=-1):
    return torch.softmax(x, dim=axis)
def _gelu(x):
    return torch.nn.functional.gelu(x, approximate="tanh")
def _silu(x):
    return torch.nn.functional.silu(x)
def _sigmoid(x):
    return torch.sigmoid(x)
def _layernorm(x, axis=-1, eps=1e-5):
    m = x.mean(dim=axis, keepdim=True)
    v = x.var(dim=axis, keepdim=True, unbiased=False)
    return (x - m) / torch.sqrt(v + eps)
def _scatter_add(tgt, idx, src, axis=0):
    return tgt.clone().index_add_(axis, idx, src)
def _segment_sum(src, seg, n):
    out = torch.zeros((n,) + src.shape[1:], dtype=src.dtype)
    return out.index_add_(0, seg, src)
def _pad(x, pw):
    flat = tuple(v for pair in reversed(pw) for v in pair)
    return torch.nn.functional.pad(x, flat)

kernel_name = 'concat_heads__linear_kernel_hedgehog__mha_block__prompt'

def setup_inputs():

    _w = (lambda a: (lambda t: t.to(torch.bfloat16) if t.is_floating_point() else t)(torch.as_tensor(a)))
    def _A_setup():
        B, S, D, H = 1, 2048, 640, 32
        Dh = D // H
        return {
            "x": _w((lambda *s: torch.randn(*s, dtype=torch.bfloat16) * 0.4)(B, S, D)),
            "wqkv": _w((lambda *s: torch.randn(*s, dtype=torch.bfloat16) * 0.4)(D, 3 * D)),
            "w_phi": _w((lambda *s: torch.randn(*s, dtype=torch.bfloat16) * 0.4)(H, Dh, Dh) * (Dh ** -0.5)),
            "wo": _w((lambda *s: torch.randn(*s, dtype=torch.bfloat16) * 0.05)(D, D)),
        }
    _A_in = _A_setup()

    def _B_setup():
        B, S, D, H = 1, 2048, 640, 32
        return {
            "x": _w((lambda *s: torch.randn(*s, dtype=torch.bfloat16) * 0.2)(B, S, D)),
            "wqkv": _w((lambda *s: torch.randn(*s, dtype=torch.bfloat16) * 0.2)(D, 3 * D)),
            "wo": _w((lambda *s: torch.randn(*s, dtype=torch.bfloat16) * 0.05)(D, D)),
        }
    _B_in = _B_setup()

    return {
        "A_x": _A_in["x"],
        "A_wqkv": _A_in["wqkv"],
        "A_w_phi": _A_in["w_phi"],
        "A_wo": _A_in["wo"],
        "B_x": _B_in["x"],
        "B_wqkv": _B_in["wqkv"],
        "B_wo": _B_in["wo"],
    }

def reference(A_x, A_wqkv, A_w_phi, A_wo, B_x, B_wqkv, B_wo):
    def _A_ref(x, wqkv, w_phi, wo):
        B, S, D = x.shape
        H = 32; Dh = D // H
        qkv = (lambda x, *d: x.permute(*d))((x @ wqkv).reshape(B, S, 3, H, Dh), 2, 0, 3, 1, 4)
        q, k, v = qkv[0], qkv[1], qkv[2]
        pq = torch.einsum("bhsd,hdf->bhsf", q, w_phi)
        pk = torch.einsum("bhsd,hdf->bhsf", k, w_phi)
        phi_q = torch.concatenate([_softmax(pq, axis=-1), _softmax(-pq, axis=-1)], axis=-1)
        phi_k = torch.concatenate([_softmax(pk, axis=-1), _softmax(-pk, axis=-1)], axis=-1)
        kv = torch.einsum("bhsf,bhsd->bhfd", phi_k, v)
        z  = torch.einsum("bhsf->bhf", phi_k)
        num = torch.einsum("bhqf,bhfd->bhqd", phi_q, kv)
        den = torch.einsum("bhqf,bhf->bhq", phi_q, z)[..., None] + 1e-6
        o = num / den
        return (lambda x, *d: x.permute(*d))(o, 0, 2, 1, 3).reshape(B, S, D) @ wo

    def _B_ref(x, wqkv, wo):
        B, S, D = x.shape
        H = 32
        Dh = D // H
        qkv = (lambda x, *d: x.permute(*d))((x @ wqkv).reshape(B, S, 3, H, Dh), 2, 0, 3, 1, 4)
        q, k, v = qkv[0], qkv[1], qkv[2]
        scores = torch.einsum("bhqd,bhkd->bhqk", q, k) * (Dh ** -0.5)
        p = _softmax(scores, axis=-1)
        o = torch.einsum("bhqk,bhkd->bhqd", p, v)
        return (lambda x, *d: x.permute(*d))(o, 0, 2, 1, 3).reshape(B, S, D) @ wo

    a = _A_ref(A_x, A_wqkv, A_w_phi, A_wo)
    b = _B_ref(B_x, B_wqkv, B_wo)
    return torch.concatenate([a, b], axis=-1)
```

```cpp
#include <hip/hip_runtime.h>
#include <stdint.h>

typedef __bf16 bf16x8 __attribute__((ext_vector_type(8)));
typedef __bf16 bf16x2v __attribute__((ext_vector_type(2)));
typedef short s16x4 __attribute__((ext_vector_type(4)));
typedef short s16x8 __attribute__((ext_vector_type(8)));
typedef float f32x2 __attribute__((ext_vector_type(2)));
typedef float f32x4 __attribute__((ext_vector_type(4)));
typedef float f32x16 __attribute__((ext_vector_type(16)));
typedef unsigned u32x2 __attribute__((ext_vector_type(2)));
typedef unsigned u32x4 __attribute__((ext_vector_type(4)));
typedef __attribute__((address_space(3))) char* lptr;
#define GAS __attribute__((address_space(1)))
typedef const GAS char* gptr;
#define DEV __device__ __forceinline__
constexpr int KVB = 10368;
constexpr size_t WS_KV = 0;
constexpr size_t WS_Q = WS_KV + 512 * KVB;
constexpr size_t WS_O = WS_Q + 5242880;
constexpr int KVPS = 3456;
constexpr size_t WS_KVP = WS_O + 5242880;
constexpr size_t WS_WOF = WS_KVP + 512 * KVPS;
constexpr size_t WS_CTL = WS_WOF + 1638400;
constexpr int FS = 64, CTL_FB = 128, CTL_FA = CTL_FB + 512 * FS, CTL_FO = CTL_FA + 128 * FS, CTL_FW = CTL_FO + 256 * FS;
constexpr float LOG2E = 1.4426950408889634f;
constexpr float QSCALE = 0.22360679774997896f * LOG2E;

DEV unsigned pk2(float a, float b) { f32x2 v = {a, b}; return __builtin_bit_cast(unsigned, __builtin_convertvector(v, bf16x2v)); }
DEV void glds16m(const void* sbase, unsigned voff, unsigned ldsdst) {
    asm volatile("s_mov_b32 m0, %2\n\ts_nop 0\n\tglobal_load_lds_dwordx4 %0, %1" :: "v"(voff), "s"(sbase), "s"(ldsdst) : "memory");
}
DEV void st16(const void* sbase, unsigned voff, u32x4 v, const int off = 0) {
    asm volatile("global_store_dwordx4 %0, %1, %2 offset:%3 sc1\n\ts_nop 1" :: "v"(voff), "v"(v), "s"(sbase), "n"(off) : "memory"); }
DEV void st8(const void* sbase, unsigned voff, u32x2 v) { asm volatile("global_store_dwordx2 %0, %1, %2 sc1" :: "v"(voff), "v"(v), "s"(sbase) : "memory"); }
DEV void st16out(const void* sbase, unsigned voff, u32x4 v) { asm volatile("global_store_dwordx4 %0, %1, %2\n\ts_nop 1" :: "v"(voff), "v"(v), "s"(sbase) : "memory"); }
DEV void chunk4k(const void* sbase, unsigned voff, unsigned ldsdst) {
    asm volatile("s_mov_b32 m0, %2\n\ts_nop 0\n\tglobal_load_lds_dwordx4 %0, %1 sc1\n\tglobal_load_lds_dwordx4 %0, %1 offset:1024 sc1\n\t"
                 "global_load_lds_dwordx4 %0, %1 offset:2048 sc1\n\tglobal_load_lds_dwordx4 %0, %1 offset:3072 sc1" :: "v"(voff), "s"(sbase), "s"(ldsdst) : "memory");
}
DEV void chunk1k(const void* sbase, unsigned voff, unsigned ldsdst) {
    asm volatile("s_mov_b32 m0, %2\n\ts_nop 0\n\tglobal_load_lds_dwordx4 %0, %1 sc1" :: "v"(voff), "s"(sbase), "s"(ldsdst) : "memory");
}
DEV void post(const char* ctl, unsigned off, unsigned long long tag) { asm volatile("global_store_dwordx2 %0, %1, %2 sc1" :: "v"(off), "v"(tag), "s"(ctl) : "memory"); }
DEV void await(const char* ctl, unsigned off, unsigned long long tag) {
#pragma unroll 1
    for (int n = 0; n < (1 << 18); ++n) { unsigned long long v;
        asm volatile("global_load_dwordx2 %0, %1, %2 sc1\n\ts_waitcnt vmcnt(0)" : "=&v"(v) : "v"(off), "s"(ctl) : "memory");
        if (__all(v == tag)) break;
        __builtin_amdgcn_s_sleep(1); }
}
#define LST16(p, ...) (*(__attribute__((address_space(3))) u32x4*)(p) = (__VA_ARGS__))
#define LST8(p, ...) (*(__attribute__((address_space(3))) u32x2*)(p) = (__VA_ARGS__))
#define LLD16(p) (*(__attribute__((address_space(3))) u32x4*)(p))
DEV s16x4 trrd(lptr p) { return __builtin_amdgcn_ds_read_tr16_b64_v4i16((__attribute__((address_space(3))) s16x4*)p); }
DEV bf16x8 cat8(s16x4 a, s16x4 b) { s16x8 r = {a[0], a[1], a[2], a[3], b[0], b[1], b[2], b[3]}; return __builtin_bit_cast(bf16x8, r); }
DEV s16x4 ld4(lptr p) { return *(__attribute__((address_space(3))) s16x4*)p; }
DEV f32x4 mf16(bf16x8 a, bf16x8 b, f32x4 c) { return __builtin_amdgcn_mfma_f32_16x16x32_bf16(a, b, c, 0, 0, 0); }
DEV f32x16 mf32(bf16x8 a, bf16x8 b, f32x16 c) { return __builtin_amdgcn_mfma_f32_32x32x16_bf16(a, b, c, 0, 0, 0); }
DEV f32x16 mf32k8(s16x4 a, s16x4 b, f32x16 c) {
    f32x16 d = __builtin_amdgcn_mfma_f32_32x32x8bf16_1k(a, b, c, 0, 0, 0); asm volatile("s_nop 9" : "+v"(d)); return d; }
DEV float max3f(float a, float b, float c) { return __builtin_fmaxf(__builtin_fmaxf(a, b), c); }
DEV float min3f(float a, float b, float c) { return __builtin_fminf(__builtin_fminf(a, b), c); }
DEV float other_half(float x) {
    const unsigned u = __float_as_uint(x); auto r = __builtin_amdgcn_permlane32_swap(u, u, false, false);
    return __uint_as_float((threadIdx.x & 32) ? r[0] : r[1]); }

constexpr int K1_W = 15360, K1_XB = 7 * K1_W, K1_X = 16384, K1_WPHI = K1_XB + 3 * K1_X, K1_ZERO = K1_WPHI + 4096, K1_LDS = 163840, K1_PF = K1_ZERO + 1024;
constexpr int K1_PH = 0, K1_VL = 4 * 128 * 80, K1_RS = 5248;
typedef float f32x8 __attribute__((ext_vector_type(8)));
typedef unsigned long long u64x4 __attribute__((ext_vector_type(4)));
typedef unsigned u32x6 __attribute__((ext_vector_type(6)));

constexpr int K2_TMP = 147456, K2_WP = K2_TMP + 2048 + 512, K2_WR = 153600, K3_WO = 2 * KVB, K3_STG = K3_WO + 102400;

__global__ __launch_bounds__(512, 2) void hedgehog_mha_fused(const char* xA_, const char* wA_, const char* wphi_, const char* xB_, const char* wB_, const char* woA_, const char* woB_, char* ws_, char* out_) {
    u64x4 ka0, ka1; unsigned long long ka2;
    asm volatile("s_load_dwordx8 %0, %3, 0x0\n\ts_load_dwordx8 %1, %3, 0x20\n\ts_load_dwordx2 %2, %3, 0x40" : "=&s"(ka0), "=&s"(ka1), "=&s"(ka2) : "s"(__builtin_amdgcn_kernarg_segment_ptr()));
    extern __shared__ __attribute__((aligned(16))) char smem[];
    lptr lds = (lptr)smem; const unsigned ldsb = (unsigned)(uintptr_t)lds;
    {
        unsigned long long pc; asm volatile("s_getpc_b64 %0" : "=s"(pc));
        asm volatile("s_mov_b32 m0, %2\n\ts_nop 0\n\tglobal_load_lds_dword %0, %1" :: "v"((((blockIdx.x >> 3) & 7) * 28 + (threadIdx.x >> 6) * 4 + (threadIdx.x & 3)) % 224 * 128), "s"(pc & ~127ull), "s"(ldsb + K1_PF) : "memory"); }
    const int w = __builtin_amdgcn_readfirstlane(threadIdx.x >> 6), c = w & 3;
    int l, lq, lp, g, tau;
#define LANES() do { int t_ = threadIdx.x; asm volatile("" : "+v"(t_)); l = t_ & 63; lq = (l & 15) >> 2; lp = l & 3; g = l >> 4; tau = l & 15; } while (0)
    LANES();
    const int xcd = blockIdx.x & 7, slot = blockIdx.x >> 3;
    const int br = xcd >> 2, ti = ((xcd >> 1) & 1) * 8 + (slot >> 2), j = (xcd & 1) * 4 + (slot & 3);
    const bool isA = br == 0;
    const int par = l >= 30, cc = l - 30 * par;
    unsigned vw = (8 * (c >> 1) + 2 * (c & 1) + par) * 3840 + cc * 16 + (isA ? j * 160 : j * 480) + (cc >= 10 ? (isA ? 1120 : 0) : 0) + (cc >= 20 ? (isA ? 1120 : 0) : 0);
    unsigned vx = (8 * c + (l >> 3)) * 1280 + (((l & 7) ^ ((l >> 4) | ((c & 1) << 2))) << 4);
    int ku = ((ti & 7) + ((slot & 3) * 5 + 1) / 2) % 10;
    unsigned xo = ti * 128 * 1280 + ku * 128, wo_ = ku * 64 * 3840;
    asm volatile("" : "+v"(vw), "+v"(vx));
    asm volatile("s_waitcnt lgkmcnt(0)" : "+s"(ka0), "+s"(ka1), "+s"(ka2), "+s"(ku), "+s"(xo), "+s"(wo_));
    const char *xA = (const char*)ka0[0], *wA = (const char*)ka0[1], *wphi = (const char*)ka0[2], *xB = (const char*)ka0[3], *wB = (const char*)ka1[0], *woA = (const char*)ka1[1], *woB = (const char*)ka1[2];
    char *ws = (char*)ka1[3], *out = (char*)ka2;
    const char* ctl = ws + WS_CTL;
    auto region = [&](int rg) -> const char* {
        const int qd = 3 * j + (rg >> 2), type = qd >> 3, head = 4 * (qd & 7) + (rg & 3);
        return ws + (type ? WS_KV + (size_t)(head * 16 + ti) * KVB + (type - 1) * 5120 : WS_Q + 2621440 + (size_t)(head * 64 + ti * 4) * 1280); };
    const char* dlast;
    u32x4 kb0 = {0, 0, 0, 0}, kb1 = kb0, qb0 = kb0, qb1 = kb0, kq2 = kb0; unsigned long long tag;
    if (__builtin_expect(w >= 4, 1)) {
        const char* xs = (isA ? xA : xB) + xo; const char* wsrc = (isA ? wA : wB) + wo_;
        const unsigned vw1 = vw + 16 * 3840, vw2 = vw + 4 * 3840, vw3 = vw + 20 * 3840, vx1 = vx + 40960, vx2 = vx + 81920, vx3 = vx + 122880;
        unsigned we = ldsb + c * 960, wo = we + K1_W, xd = ldsb + K1_XB + c * 1024;
        int nw = 0;
#pragma unroll 1
        for (int n = 0; n < 10; ++n) {
#define K1_LD(v, s) "s_nop 0\n\tglobal_load_lds_dwordx4 " v ", " s "\n\t"
            asm volatile("s_mov_b32 exec_hi, 0x0fffffff\n\t"
                         "s_mov_b32 m0, %11\n\t" K1_LD("%0", "%8") "s_add_u32 m0, m0, 3840\n\t" K1_LD("%1", "%8") "s_add_u32 m0, m0, 3840\n\t" K1_LD("%2", "%8") "s_add_u32 m0, m0, 3840\n\t" K1_LD("%3", "%8")
                         "s_mov_b32 m0, %12\n\t" K1_LD("%0", "%9") "s_add_u32 m0, m0, 3840\n\t" K1_LD("%1", "%9") "s_add_u32 m0, m0, 3840\n\t" K1_LD("%2", "%9") "s_add_u32 m0, m0, 3840\n\t" K1_LD("%3", "%9")
                         "s_mov_b32 exec_hi, -1\n\t"
                         "s_mov_b32 m0, %13\n\t" K1_LD("%4", "%10") "s_add_u32 m0, m0, 4096\n\t" K1_LD("%5", "%10") "s_add_u32 m0, m0, 4096\n\t" K1_LD("%6", "%10") "s_add_u32 m0, m0, 4096\n\t" K1_LD("%7", "%10")
                         :: "v"(vw), "v"(vw1), "v"(vw2), "v"(vw3), "v"(vx), "v"(vx1), "v"(vx2), "v"(vx3), "s"(wsrc), "s"(wsrc + 32 * 3840), "s"(xs), "s"(we), "s"(wo), "s"(xd) : "memory", "scc");
            ++ku; wsrc += ku == 10 ? -9 * 64 * 3840 : 64 * 3840; xs += ku == 10 ? -9 * 128 : 128;
            nw += 2; we += nw >= 7 ? -5 * K1_W : 2 * K1_W; wo += (nw | 1) == 7 ? -5 * K1_W : 2 * K1_W; nw -= nw >= 7 ? 7 : 0;
            xd += ((0x124 >> n) & 1) ? -2 * K1_X : K1_X;
            if (n >= 1) asm volatile("s_waitcnt vmcnt(12)\n\ts_barrier" ::: "memory");
            if (n == 8) { const int q = c * 64 + l; glds16m(wphi + j * 3200, (q < 200 ? q : 199) * 16, ldsb + K1_WPHI + c * 1024); }
        }
        dlast = region(8 + (w >> 1)) + (w & 1) * 3072;
        asm volatile("global_load_dwordx2 %0, %1, %2 sc1\n\ts_waitcnt vmcnt(0)\n\ts_barrier\n\ts_barrier" : "=&v"(tag) : "v"(0), "s"(ctl) : "memory");
        if (!isA) {
            const char* d0 = region(c); const char* d1 = region(4 + c); const bool v0 = 3 * j >= 16, v1 = 3 * j + 1 >= 16;
#pragma unroll 1
            for (int u = 0; u < 2; ++u) {
                asm volatile("s_barrier" ::: "memory");
                lptr src = lds + (4 * u + c) * K1_RS + l * 16; u32x4 x[6];
#pragma unroll
                for (int i = 0; i < 6; ++i) x[i] = LLD16(src + 1024 * i);
#pragma unroll
                for (int i = 0; i < 5; ++i) st16(d0, l * 16 + (i == 4 ? 4096 : 0), x[i], (i & 3) * 1024);
                if (l < ((u ? v1 : v0) ? 8 : 0)) st16(d0, l * 16 + 4096, x[5], 1024);
                d0 = d1;
            }
            asm volatile("s_barrier" ::: "memory");
        }
    } else {
        if (c == 0) *(__attribute__((address_space(3))) u32x4*)(lds + K1_ZERO + l * 16) = u32x4{0, 0, 0, 0};
        dlast = region(8 + (w >> 1)) + (w & 1) * 3072;
        unsigned awn = ldsb + (4 * g + lq) * 480 + 40 * lp, awv = ldsb + (4 * g + lq) * 480 + 8 * lp;
        unsigned ax0 = ldsb + K1_XB + (c * 32 + tau) * 128 + ((g ^ (tau >> 1)) << 4), ax1 = ax0 ^ 64, it, sl, dl, xd; unsigned long long sc;
        const unsigned rk = 8 * (tau >> 2) + (tau & 3), rb = ldsb + g * K1_RS;
        const unsigned oq1 = rb + c * 1280 + tau * 16, oq2 = rb + c * 1280 + 1024 + tau * 8, ok1 = rb + c * 1024 + rk * 16, ok2 = rb + 4096 + ((c & 1) * 32 + rk) * 16 + (c >> 1) * 8;
        const u32x4 adq = {oq1, oq1 + 256, oq2, oq2 + 128}, adk = {ok1, ok1 + 64, ok2, ok2 + 64};
        const unsigned vb = ldsb + (2 * c + (g >> 1)) * 656, vo = vb + (g & 1) * 320 + tau * 16;
        u32x6 adv = {vo, vo + 256 + (tau >= 4 ? K1_RS - 320 : 0), vo + 512 + (tau >= 8 ? 2 : 1) * (K1_RS - 320), vo + 768 + (tau >= 12 ? 3 : 2) * (K1_RS - 320), vo + 1024 + 3 * (K1_RS - 320), vb + tau * K1_RS + 640};
        if (isA) { adv[0] = ldsb + K1_XB + c * 5120 + l * 16; adv[1] = ldsb + K1_VL + (g * 128 + c * 32 + tau) * 48; }
#define K1_LOOP(TEXT) asm volatile(TEXT \
            : "=&{v[80:83]}"(kb0), "=&{v[84:87]}"(kb1), "=&{v[88:91]}"(qb0), "=&{v[92:95]}"(qb1), "=&{v[96:99]}"(kq2), "=&v"(tag), \
              "+v"(awn), "+v"(awv), "+v"(ax0), "+v"(ax1), "=&s"(it), "=&s"(sl), "=&s"(dl), "=&s"(xd), "=&s"(sc) \
            : "s"(K1_X), "s"(-2 * K1_X), "s"(3 * j), "s"(br), "s"(ctl), "{v[64:67]}"(adq), "{v[68:71]}"(adk), "{v[72:77]}"(adv) : "memory", "scc", "vcc", "v100", "v101", "v102", "v103", "v104", "v105", "v106", "v107", "v108", "v109", "v110", "v111", "v112", "v113", "v114", "v115", "v116", "v117", "v118", "v119", "v120", "v121", "v122", "v123", "v124", "v125", "v126", "v127", "v128", "v129", "v130", "v131", "v132", "v133", "v134", "v135", "v136", "v137", "v138", "v139", "v140", "v141", "v142", "v143", "v144", "v145", "v146", "v147", "v148", "v149", "v150", "v151", "v152", "v153", "v154", "v155", "v156", "v157", "v158", "v159", "v160", "v161", "v162", "v163", "v164", "v165", "v166", "v167", "v168", "v169", "v170", "v171", "v172", "v173", "v174", "v175", "v176", "v177", "v178", "v179", "v180", "v181", "v182", "v183", "v184", "v185", "v186", "v187", "v188", "v189", "v190", "v191", "v192", "v193", "v194", "v195", "v196", "v197", "v198", "v199", "v200", "v201", "v202", "v203", "v204", "v205", "v206", "v207", "v208", "v209", "v210", "v211", "v212", "v213", "v214", "v215", "v216", "v217", "v218", "v219", "v220", "v221", "v222", "v223", "v224", "v225", "v226", "v227", "v228", "v229", "v230", "v231", "v232", "v233", "v234", "v235", "v236", "v237", "v238", "v239", "v240", "v241", "v242", "v243", "v244", "v245", "v246", "v247")
        if (isA || j < 5) K1_LOOP(
    "v_mov_b32 v120, 0\n"
    "global_load_dwordx2 %5, v120, %19 sc1\n"
    "v_mov_b64 v[128:129], 0\n"
    "v_mov_b64 v[130:131], 0\n"
    "v_mov_b64 v[132:133], 0\n"
    "v_mov_b64 v[134:135], 0\n"
    "v_mov_b64 v[136:137], 0\n"
    "v_mov_b64 v[138:139], 0\n"
    "v_mov_b64 v[140:141], 0\n"
    "v_mov_b64 v[142:143], 0\n"
    "v_mov_b64 v[144:145], 0\n"
    "v_mov_b64 v[146:147], 0\n"
    "v_mov_b64 v[148:149], 0\n"
    "v_mov_b64 v[150:151], 0\n"
    "v_mov_b64 v[152:153], 0\n"
    "v_mov_b64 v[154:155], 0\n"
    "v_mov_b64 v[156:157], 0\n"
    "v_mov_b64 v[158:159], 0\n"
    "v_mov_b64 v[160:161], 0\n"
    "v_mov_b64 v[162:163], 0\n"
    "v_mov_b64 v[164:165], 0\n"
    "v_mov_b64 v[166:167], 0\n"
    "v_mov_b64 v[168:169], 0\n"
    "v_mov_b64 v[170:171], 0\n"
    "v_mov_b64 v[172:173], 0\n"
    "v_mov_b64 v[174:175], 0\n"
    "v_mov_b64 v[176:177], 0\n"
    "v_mov_b64 v[178:179], 0\n"
    "v_mov_b64 v[180:181], 0\n"
    "v_mov_b64 v[182:183], 0\n"
    "v_mov_b64 v[184:185], 0\n"
    "v_mov_b64 v[186:187], 0\n"
    "v_mov_b64 v[188:189], 0\n"
    "v_mov_b64 v[190:191], 0\n"
    "v_mov_b64 v[192:193], 0\n"
    "v_mov_b64 v[194:195], 0\n"
    "v_mov_b64 v[196:197], 0\n"
    "v_mov_b64 v[198:199], 0\n"
    "v_mov_b64 v[200:201], 0\n"
    "v_mov_b64 v[202:203], 0\n"
    "v_mov_b64 v[204:205], 0\n"
    "v_mov_b64 v[206:207], 0\n"
    "v_mov_b64 v[208:209], 0\n"
    "v_mov_b64 v[210:211], 0\n"
    "v_mov_b64 v[212:213], 0\n"
    "v_mov_b64 v[214:215], 0\n"
    "v_mov_b64 v[216:217], 0\n"
    "v_mov_b64 v[218:219], 0\n"
    "v_mov_b64 v[220:221], 0\n"
    "v_mov_b64 v[222:223], 0\n"
    "v_mov_b64 v[224:225], 0\n"
    "v_mov_b64 v[226:227], 0\n"
    "v_mov_b64 v[228:229], 0\n"
    "v_mov_b64 v[230:231], 0\n"
    "v_mov_b64 v[232:233], 0\n"
    "v_mov_b64 v[234:235], 0\n"
    "v_mov_b64 v[236:237], 0\n"
    "v_mov_b64 v[238:239], 0\n"
    "v_mov_b64 v[240:241], 0\n"
    "v_mov_b64 v[242:243], 0\n"
    "v_mov_b64 v[244:245], 0\n"
    "v_mov_b64 v[246:247], 0\n"
    "s_waitcnt lgkmcnt(0)\n"
    "s_barrier\n"
    "s_mov_b32 %10, 0\n"
    "s_mov_b32 %11, 0\n"
    "ds_read_b128 v[80:83], %8\n"
    "ds_read_b128 v[84:87], %8 offset:2048\n"
    "ds_read_b64_tr_b16 v[96:97], %6 offset:0\n"
    "ds_read_b64_tr_b16 v[98:99], %6 offset:7680\n"
    "ds_read_b64_tr_b16 v[100:101], %6 offset:8\n"
    "ds_read_b64_tr_b16 v[102:103], %6 offset:7688\n"
    "ds_read_b64_tr_b16 v[104:105], %6 offset:16\n"
    "ds_read_b64_tr_b16 v[106:107], %6 offset:7696\n"
    "ds_read_b64_tr_b16 v[108:109], %6 offset:24\n"
    "ds_read_b64_tr_b16 v[110:111], %6 offset:7704\n"
    "ds_read_b64_tr_b16 v[112:113], %6 offset:32\n"
    "ds_read_b64_tr_b16 v[114:115], %6 offset:7712\n"
    "s_waitcnt lgkmcnt(8)\n"
    ".p2align 6\n"
    "1:\n"
    "v_mfma_f32_16x16x32_bf16 v[128:131], v[96:99], v[80:83], v[128:131]\n"
    "ds_read_b64_tr_b16 v[116:117], %6 offset:160\n"
    "v_mfma_f32_16x16x32_bf16 v[132:135], v[96:99], v[84:87], v[132:135]\n"
    "s_waitcnt lgkmcnt(6)\n"
    "ds_read_b64_tr_b16 v[118:119], %6 offset:7840\n"
    "v_mfma_f32_16x16x32_bf16 v[136:139], v[100:103], v[80:83], v[136:139]\n"
    "ds_read_b64_tr_b16 v[96:97], %6 offset:168\n"
    "v_mfma_f32_16x16x32_bf16 v[140:143], v[100:103], v[84:87], v[140:143]\n"
    "s_waitcnt lgkmcnt(7)\n"
    "ds_read_b64_tr_b16 v[98:99], %6 offset:7848\n"
    "v_mfma_f32_16x16x32_bf16 v[144:147], v[104:107], v[80:83], v[144:147]\n"
    "s_add_u32 %11, %11, 1\n"
    "ds_read_b64_tr_b16 v[100:101], %6 offset:176\n"
    "v_mfma_f32_16x16x32_bf16 v[148:151], v[104:107], v[84:87], v[148:151]\n"
    "s_waitcnt lgkmcnt(7)\n"
    "ds_read_b64_tr_b16 v[102:103], %6 offset:7856\n"
    "v_mfma_f32_16x16x32_bf16 v[152:155], v[108:111], v[80:83], v[152:155]\n"
    "s_mov_b32 %12, 15360\n"
    "ds_read_b64_tr_b16 v[104:105], %6 offset:184\n"
    "v_mfma_f32_16x16x32_bf16 v[156:159], v[108:111], v[84:87], v[156:159]\n"
    "s_waitcnt lgkmcnt(7)\n"
    "ds_read_b64_tr_b16 v[106:107], %6 offset:7864\n"
    "v_mfma_f32_16x16x32_bf16 v[160:163], v[112:115], v[80:83], v[160:163]\n"
    "s_cmp_eq_u32 %11, 7\n"
    "ds_read_b64_tr_b16 v[108:109], %6 offset:192\n"
    "v_mfma_f32_16x16x32_bf16 v[164:167], v[112:115], v[84:87], v[164:167]\n"
    "s_waitcnt lgkmcnt(7)\n"
    "ds_read_b64_tr_b16 v[110:111], %6 offset:7872\n"
    "v_mfma_f32_16x16x32_bf16 v[168:171], v[116:119], v[80:83], v[168:171]\n"
    "s_cmov_b32 %12, -92160\n"
    "ds_read_b64_tr_b16 v[112:113], %6 offset:320\n"
    "v_mfma_f32_16x16x32_bf16 v[172:175], v[116:119], v[84:87], v[172:175]\n"
    "s_waitcnt lgkmcnt(7)\n"
    "ds_read_b64_tr_b16 v[114:115], %6 offset:8000\n"
    "v_mfma_f32_16x16x32_bf16 v[176:179], v[96:99], v[80:83], v[176:179]\n"
    "s_cmov_b32 %11, 0\n"
    "ds_read_b64_tr_b16 v[116:117], %6 offset:328\n"
    "v_mfma_f32_16x16x32_bf16 v[180:183], v[96:99], v[84:87], v[180:183]\n"
    "s_waitcnt lgkmcnt(7)\n"
    "ds_read_b64_tr_b16 v[118:119], %6 offset:8008\n"
    "v_mfma_f32_16x16x32_bf16 v[184:187], v[100:103], v[80:83], v[184:187]\n"
    "ds_read_b64_tr_b16 v[96:97], %6 offset:336\n"
    "v_mfma_f32_16x16x32_bf16 v[188:191], v[100:103], v[84:87], v[188:191]\n"
    "s_waitcnt lgkmcnt(7)\n"
    "ds_read_b64_tr_b16 v[98:99], %6 offset:8016\n"
    "v_mfma_f32_16x16x32_bf16 v[192:195], v[104:107], v[80:83], v[192:195]\n"
    "ds_read_b64_tr_b16 v[100:101], %6 offset:344\n"
    "v_mfma_f32_16x16x32_bf16 v[196:199], v[104:107], v[84:87], v[196:199]\n"
    "s_waitcnt lgkmcnt(7)\n"
    "ds_read_b64_tr_b16 v[102:103], %6 offset:8024\n"
    "v_mfma_f32_16x16x32_bf16 v[200:203], v[108:111], v[80:83], v[200:203]\n"
    "ds_read_b64_tr_b16 v[104:105], %6 offset:352\n"
    "v_mfma_f32_16x16x32_bf16 v[204:207], v[108:111], v[84:87], v[204:207]\n"
    "s_waitcnt lgkmcnt(7)\n"
    "ds_read_b64_tr_b16 v[106:107], %6 offset:8032\n"
    "v_add_u32 %6, %12, %6\n"
    "v_mfma_f32_16x16x32_bf16 v[208:211], v[112:115], v[80:83], v[208:211]\n"
    "ds_read_b64_tr_b16 v[108:109], %6 offset:0\n"
    "v_mfma_f32_16x16x32_bf16 v[212:215], v[112:115], v[84:87], v[212:215]\n"
    "s_waitcnt lgkmcnt(7)\n"
    "ds_read_b64_tr_b16 v[110:111], %6 offset:7680\n"
    "v_mfma_f32_16x16x32_bf16 v[216:219], v[116:119], v[80:83], v[216:219]\n"
    "ds_read_b64_tr_b16 v[112:113], %6 offset:8\n"
    "ds_read_b128 v[88:91], %9\n"
    "v_mfma_f32_16x16x32_bf16 v[220:223], v[116:119], v[84:87], v[220:223]\n"
    "s_waitcnt lgkmcnt(8)\n"
    "ds_read_b64_tr_b16 v[114:115], %6 offset:7688\n"
    "v_mfma_f32_16x16x32_bf16 v[224:227], v[96:99], v[80:83], v[224:227]\n"
    "s_add_u32 %10, %10, 1\n"
    "ds_read_b64_tr_b16 v[116:117], %6 offset:16\n"
    "ds_read_b128 v[92:95], %9 offset:2048\n"
    "v_mfma_f32_16x16x32_bf16 v[228:231], v[96:99], v[84:87], v[228:231]\n"
    "s_waitcnt lgkmcnt(9)\n"
    "ds_read_b64_tr_b16 v[118:119], %6 offset:7696\n"
    "v_mfma_f32_16x16x32_bf16 v[232:235], v[100:103], v[80:83], v[232:235]\n"
    "s_mul_i32 %13, %10, 11\n"
    "ds_read_b64_tr_b16 v[96:97], %6 offset:24\n"
    "v_mfma_f32_16x16x32_bf16 v[236:239], v[100:103], v[84:87], v[236:239]\n"
    "s_waitcnt lgkmcnt(9)\n"
    "ds_read_b64_tr_b16 v[98:99], %6 offset:7704\n"
    "v_mfma_f32_16x16x32_bf16 v[240:243], v[104:107], v[80:83], v[240:243]\n"
    "s_and_b32 %13, %13, 28\n"
    "ds_read_b64_tr_b16 v[100:101], %6 offset:32\n"
    "v_mfma_f32_16x16x32_bf16 v[244:247], v[104:107], v[84:87], v[244:247]\n"
    "s_waitcnt lgkmcnt(4)\n"
    "ds_read_b64_tr_b16 v[102:103], %6 offset:7712\n"
    "v_mfma_f32_16x16x32_bf16 v[128:131], v[108:111], v[88:91], v[128:131]\n"
    "s_cselect_b32 %13, %15, %16\n"
    "ds_read_b64_tr_b16 v[104:105], %6 offset:160\n"
    "v_mfma_f32_16x16x32_bf16 v[132:135], v[108:111], v[92:95], v[132:135]\n"
    "s_waitcnt lgkmcnt(6)\n"
    "ds_read_b64_tr_b16 v[106:107], %6 offset:7840\n"
    "v_mfma_f32_16x16x32_bf16 v[136:139], v[112:115], v[88:91], v[136:139]\n"
    "ds_read_b64_tr_b16 v[108:109], %6 offset:168\n"
    "v_mfma_f32_16x16x32_bf16 v[140:143], v[112:115], v[92:95], v[140:143]\n"
    "s_waitcnt lgkmcnt(7)\n"
    "ds_read_b64_tr_b16 v[110:111], %6 offset:7848\n"
    "v_mfma_f32_16x16x32_bf16 v[144:147], v[116:119], v[88:91], v[144:147]\n"
    "s_add_u32 %11, %11, 1\n"
    "ds_read_b64_tr_b16 v[112:113], %6 offset:176\n"
    "v_mfma_f32_16x16x32_bf16 v[148:151], v[116:119], v[92:95], v[148:151]\n"
    "s_waitcnt lgkmcnt(7)\n"
    "ds_read_b64_tr_b16 v[114:115], %6 offset:7856\n"
    "v_mfma_f32_16x16x32_bf16 v[152:155], v[96:99], v[88:91], v[152:155]\n"
    "s_mov_b32 %12, 15360\n"
    "ds_read_b64_tr_b16 v[116:117], %6 offset:184\n"
    "v_mfma_f32_16x16x32_bf16 v[156:159], v[96:99], v[92:95], v[156:159]\n"
    "s_waitcnt lgkmcnt(7)\n"
    "ds_read_b64_tr_b16 v[118:119], %6 offset:7864\n"
    "v_mfma_f32_16x16x32_bf16 v[160:163], v[100:103], v[88:91], v[160:163]\n"
    "s_cmp_eq_u32 %11, 7\n"
    "ds_read_b64_tr_b16 v[96:97], %6 offset:192\n"
    "v_mfma_f32_16x16x32_bf16 v[164:167], v[100:103], v[92:95], v[164:167]\n"
    "s_waitcnt lgkmcnt(7)\n"
    "ds_read_b64_tr_b16 v[98:99], %6 offset:7872\n"
    "v_mfma_f32_16x16x32_bf16 v[168:171], v[104:107], v[88:91], v[168:171]\n"
    "s_cmov_b32 %12, -92160\n"
    "ds_read_b64_tr_b16 v[100:101], %6 offset:320\n"
    "v_mfma_f32_16x16x32_bf16 v[172:175], v[104:107], v[92:95], v[172:175]\n"
    "s_waitcnt lgkmcnt(7)\n"
    "ds_read_b64_tr_b16 v[102:103], %6 offset:8000\n"
    "v_mfma_f32_16x16x32_bf16 v[176:179], v[108:111], v[88:91], v[176:179]\n"
    "s_cmov_b32 %11, 0\n"
    "ds_read_b64_tr_b16 v[104:105], %6 offset:328\n"
    "v_mfma_f32_16x16x32_bf16 v[180:183], v[108:111], v[92:95], v[180:183]\n"
    "s_waitcnt lgkmcnt(7)\n"
    "ds_read_b64_tr_b16 v[106:107], %6 offset:8008\n"
    "v_mfma_f32_16x16x32_bf16 v[184:187], v[112:115], v[88:91], v[184:187]\n"
    "ds_read_b64_tr_b16 v[108:109], %6 offset:336\n"
    "v_mfma_f32_16x16x32_bf16 v[188:191], v[112:115], v[92:95], v[188:191]\n"
    "s_waitcnt lgkmcnt(7)\n"
    "ds_read_b64_tr_b16 v[110:111], %6 offset:8016\n"
    "v_mfma_f32_16x16x32_bf16 v[192:195], v[116:119], v[88:91], v[192:195]\n"
    "ds_read_b64_tr_b16 v[112:113], %6 offset:344\n"
    "v_mfma_f32_16x16x32_bf16 v[196:199], v[116:119], v[92:95], v[196:199]\n"
    "s_waitcnt lgkmcnt(7)\n"
    "ds_read_b64_tr_b16 v[114:115], %6 offset:8024\n"
    "v_add_u32 %8, %13, %8\n"
    "v_mfma_f32_16x16x32_bf16 v[200:203], v[96:99], v[88:91], v[200:203]\n"
    "ds_read_b64_tr_b16 v[116:117], %6 offset:352\n"
    "v_mfma_f32_16x16x32_bf16 v[204:207], v[96:99], v[92:95], v[204:207]\n"
    "s_waitcnt lgkmcnt(7)\n"
    "ds_read_b64_tr_b16 v[118:119], %6 offset:8032\n"
    "v_add_u32 %6, %12, %6\n"
    "v_mfma_f32_16x16x32_bf16 v[208:211], v[100:103], v[88:91], v[208:211]\n"
    "s_barrier\n"
    "ds_read_b64_tr_b16 v[96:97], %6 offset:0\n"
    "v_mfma_f32_16x16x32_bf16 v[212:215], v[100:103], v[92:95], v[212:215]\n"
    "s_waitcnt lgkmcnt(7)\n"
    "ds_read_b64_tr_b16 v[98:99], %6 offset:7680\n"
    "v_mfma_f32_16x16x32_bf16 v[216:219], v[104:107], v[88:91], v[216:219]\n"
    "ds_read_b64_tr_b16 v[100:101], %6 offset:8\n"
    "ds_read_b128 v[80:83], %8\n"
    "v_mfma_f32_16x16x32_bf16 v[220:223], v[104:107], v[92:95], v[220:223]\n"
    "s_waitcnt lgkmcnt(8)\n"
    "ds_read_b64_tr_b16 v[102:103], %6 offset:7688\n"
    "v_mfma_f32_16x16x32_bf16 v[224:227], v[108:111], v[88:91], v[224:227]\n"
    "ds_read_b64_tr_b16 v[104:105], %6 offset:16\n"
    "ds_read_b128 v[84:87], %8 offset:2048\n"
    "v_mfma_f32_16x16x32_bf16 v[228:231], v[108:111], v[92:95], v[228:231]\n"
    "s_waitcnt lgkmcnt(9)\n"
    "ds_read_b64_tr_b16 v[106:107], %6 offset:7696\n"
    "v_add_u32 %9, %13, %9\n"
    "v_mfma_f32_16x16x32_bf16 v[232:235], v[112:115], v[88:91], v[232:235]\n"
    "s_cmp_lt_u32 %10, 10\n"
    "ds_read_b64_tr_b16 v[108:109], %6 offset:24\n"
    "v_mfma_f32_16x16x32_bf16 v[236:239], v[112:115], v[92:95], v[236:239]\n"
    "s_waitcnt lgkmcnt(9)\n"
    "ds_read_b64_tr_b16 v[110:111], %6 offset:7704\n"
    "v_mfma_f32_16x16x32_bf16 v[240:243], v[116:119], v[88:91], v[240:243]\n"
    "ds_read_b64_tr_b16 v[112:113], %6 offset:32\n"
    "v_mfma_f32_16x16x32_bf16 v[244:247], v[116:119], v[92:95], v[244:247]\n"
    "s_waitcnt lgkmcnt(4)\n"
    "ds_read_b64_tr_b16 v[114:115], %6 offset:7712\n"
    "s_cbranch_scc1 1b\n"
    "s_waitcnt vmcnt(0) lgkmcnt(0)\n"
    "s_nop 7\n"
    "s_cmp_eq_u32 %18, 0\n"
    "s_cbranch_scc0 4f\n"
    "v_cvt_pk_bf16_f32 v80, v172, v173\n"
    "v_cvt_pk_bf16_f32 v81, v174, v175\n"
    "v_cvt_pk_bf16_f32 v82, v180, v181\n"
    "v_cvt_pk_bf16_f32 v83, v182, v183\n"
    "v_cvt_pk_bf16_f32 v84, v188, v189\n"
    "v_cvt_pk_bf16_f32 v85, v190, v191\n"
    "v_cvt_pk_bf16_f32 v86, v196, v197\n"
    "v_cvt_pk_bf16_f32 v87, v198, v199\n"
    "v_cvt_pk_bf16_f32 v96, v204, v205\n"
    "v_cvt_pk_bf16_f32 v97, v206, v207\n"
    "ds_write_b128 v72, v[80:83] offset:0\n"
    "ds_write_b128 v72, v[84:87] offset:1024\n"
    "v_cvt_pk_bf16_f32 v88, v132, v133\n"
    "v_cvt_pk_bf16_f32 v89, v134, v135\n"
    "v_cvt_pk_bf16_f32 v90, v140, v141\n"
    "v_cvt_pk_bf16_f32 v91, v142, v143\n"
    "v_cvt_pk_bf16_f32 v92, v148, v149\n"
    "v_cvt_pk_bf16_f32 v93, v150, v151\n"
    "v_cvt_pk_bf16_f32 v94, v156, v157\n"
    "v_cvt_pk_bf16_f32 v95, v158, v159\n"
    "v_cvt_pk_bf16_f32 v98, v164, v165\n"
    "v_cvt_pk_bf16_f32 v99, v166, v167\n"
    "ds_write_b128 v72, v[88:91] offset:2048\n"
    "ds_write_b128 v72, v[92:95] offset:3072\n"
    "ds_write_b128 v72, v[96:99] offset:4096\n"
    "v_cvt_pk_bf16_f32 v100, v208, v209\n"
    "v_cvt_pk_bf16_f32 v101, v210, v211\n"
    "v_cvt_pk_bf16_f32 v102, v216, v217\n"
    "v_cvt_pk_bf16_f32 v103, v218, v219\n"
    "v_cvt_pk_bf16_f32 v104, v224, v225\n"
    "v_cvt_pk_bf16_f32 v105, v226, v227\n"
    "v_cvt_pk_bf16_f32 v106, v232, v233\n"
    "v_cvt_pk_bf16_f32 v107, v234, v235\n"
    "v_cvt_pk_bf16_f32 v108, v240, v241\n"
    "v_cvt_pk_bf16_f32 v109, v242, v243\n"
    "v_mov_b32 v110, 0x3f80\n"
    "v_mov_b32 v111, 0\n"
    "s_waitcnt lgkmcnt(0)\n"
    "s_barrier\n"
    "ds_write_b128 v73, v[100:103] offset:0\n"
    "ds_write_b128 v73, v[104:107] offset:16\n"
    "ds_write_b128 v73, v[108:111] offset:32\n"
    "v_cvt_pk_bf16_f32 v112, v212, v213\n"
    "v_cvt_pk_bf16_f32 v113, v214, v215\n"
    "v_cvt_pk_bf16_f32 v114, v220, v221\n"
    "v_cvt_pk_bf16_f32 v115, v222, v223\n"
    "v_cvt_pk_bf16_f32 v116, v228, v229\n"
    "v_cvt_pk_bf16_f32 v117, v230, v231\n"
    "v_cvt_pk_bf16_f32 v118, v236, v237\n"
    "v_cvt_pk_bf16_f32 v119, v238, v239\n"
    "v_cvt_pk_bf16_f32 v120, v244, v245\n"
    "v_cvt_pk_bf16_f32 v121, v246, v247\n"
    "v_mov_b32 v122, 0x3f80\n"
    "v_mov_b32 v123, 0\n"
    "ds_write_b128 v73, v[112:115] offset:768\n"
    "ds_write_b128 v73, v[116:119] offset:784\n"
    "ds_write_b128 v73, v[120:123] offset:800\n"
    "v_cvt_pk_bf16_f32 v88, v128, v129\n"
    "v_cvt_pk_bf16_f32 v89, v130, v131\n"
    "v_cvt_pk_bf16_f32 v90, v136, v137\n"
    "v_cvt_pk_bf16_f32 v91, v138, v139\n"
    "v_cvt_pk_bf16_f32 v92, v144, v145\n"
    "v_cvt_pk_bf16_f32 v93, v146, v147\n"
    "v_cvt_pk_bf16_f32 v94, v152, v153\n"
    "v_cvt_pk_bf16_f32 v95, v154, v155\n"
    "v_cvt_pk_bf16_f32 v98, v160, v161\n"
    "v_cvt_pk_bf16_f32 v99, v162, v163\n"
    "v_cvt_pk_bf16_f32 v80, v168, v169\n"
    "v_cvt_pk_bf16_f32 v81, v170, v171\n"
    "v_cvt_pk_bf16_f32 v82, v176, v177\n"
    "v_cvt_pk_bf16_f32 v83, v178, v179\n"
    "v_cvt_pk_bf16_f32 v84, v184, v185\n"
    "v_cvt_pk_bf16_f32 v85, v186, v187\n"
    "v_cvt_pk_bf16_f32 v86, v192, v193\n"
    "v_cvt_pk_bf16_f32 v87, v194, v195\n"
    "v_cvt_pk_bf16_f32 v96, v200, v201\n"
    "v_cvt_pk_bf16_f32 v97, v202, v203\n"
    "s_branch 5f\n"
    "4:\n"
    "s_mov_b64 %14, 0x3ea52b5b\n"
    "s_cmp_lt_u32 %17, 8\n"
    "s_cselect_b64 vcc, -1, 0\n"
    "s_cbranch_scc0 6f\n"
    "v_pk_mul_f32 v[128:129], v[128:129], %14 op_sel_hi:[1,0]\n"
    "v_pk_mul_f32 v[130:131], v[130:131], %14 op_sel_hi:[1,0]\n"
    "v_pk_mul_f32 v[132:133], v[132:133], %14 op_sel_hi:[1,0]\n"
    "v_pk_mul_f32 v[134:135], v[134:135], %14 op_sel_hi:[1,0]\n"
    "v_pk_mul_f32 v[136:137], v[136:137], %14 op_sel_hi:[1,0]\n"
    "v_pk_mul_f32 v[138:139], v[138:139], %14 op_sel_hi:[1,0]\n"
    "v_pk_mul_f32 v[140:141], v[140:141], %14 op_sel_hi:[1,0]\n"
    "v_pk_mul_f32 v[142:143], v[142:143], %14 op_sel_hi:[1,0]\n"
    "v_pk_mul_f32 v[144:145], v[144:145], %14 op_sel_hi:[1,0]\n"
    "v_pk_mul_f32 v[146:147], v[146:147], %14 op_sel_hi:[1,0]\n"
    "v_pk_mul_f32 v[148:149], v[148:149], %14 op_sel_hi:[1,0]\n"
    "v_pk_mul_f32 v[150:151], v[150:151], %14 op_sel_hi:[1,0]\n"
    "v_pk_mul_f32 v[152:153], v[152:153], %14 op_sel_hi:[1,0]\n"
    "v_pk_mul_f32 v[154:155], v[154:155], %14 op_sel_hi:[1,0]\n"
    "v_pk_mul_f32 v[156:157], v[156:157], %14 op_sel_hi:[1,0]\n"
    "v_pk_mul_f32 v[158:159], v[158:159], %14 op_sel_hi:[1,0]\n"
    "v_pk_mul_f32 v[160:161], v[160:161], %14 op_sel_hi:[1,0]\n"
    "v_pk_mul_f32 v[162:163], v[162:163], %14 op_sel_hi:[1,0]\n"
    "v_pk_mul_f32 v[164:165], v[164:165], %14 op_sel_hi:[1,0]\n"
    "v_pk_mul_f32 v[166:167], v[166:167], %14 op_sel_hi:[1,0]\n"
    "6:\n"
    "v_cndmask_b32 v120, v68, v64, vcc\n"
    "v_cndmask_b32 v121, v69, v65, vcc\n"
    "v_cndmask_b32 v122, v70, v66, vcc\n"
    "v_cndmask_b32 v123, v71, v67, vcc\n"
    "v_cvt_pk_bf16_f32 v80, v128, v129\n"
    "v_cvt_pk_bf16_f32 v81, v130, v131\n"
    "v_cvt_pk_bf16_f32 v82, v136, v137\n"
    "v_cvt_pk_bf16_f32 v83, v138, v139\n"
    "v_cvt_pk_bf16_f32 v84, v144, v145\n"
    "v_cvt_pk_bf16_f32 v85, v146, v147\n"
    "v_cvt_pk_bf16_f32 v86, v152, v153\n"
    "v_cvt_pk_bf16_f32 v87, v154, v155\n"
    "v_cvt_pk_bf16_f32 v88, v160, v161\n"
    "v_cvt_pk_bf16_f32 v89, v162, v163\n"
    "ds_write_b128 v120, v[80:83] offset:0\n"
    "ds_write_b128 v120, v[84:87] offset:512\n"
    "ds_write_b64 v122, v[88:89] offset:0\n"
    "v_cvt_pk_bf16_f32 v90, v132, v133\n"
    "v_cvt_pk_bf16_f32 v91, v134, v135\n"
    "v_cvt_pk_bf16_f32 v92, v140, v141\n"
    "v_cvt_pk_bf16_f32 v93, v142, v143\n"
    "v_cvt_pk_bf16_f32 v94, v148, v149\n"
    "v_cvt_pk_bf16_f32 v95, v150, v151\n"
    "v_cvt_pk_bf16_f32 v96, v156, v157\n"
    "v_cvt_pk_bf16_f32 v97, v158, v159\n"
    "v_cvt_pk_bf16_f32 v98, v164, v165\n"
    "v_cvt_pk_bf16_f32 v99, v166, v167\n"
    "ds_write_b128 v121, v[90:93] offset:0\n"
    "ds_write_b128 v121, v[94:97] offset:512\n"
    "ds_write_b64 v123, v[98:99] offset:0\n"
    "s_waitcnt lgkmcnt(0)\n"
    "s_barrier\n"
    "s_cmp_lt_u32 %17, 7\n"
    "s_cselect_b64 vcc, -1, 0\n"
    "s_cbranch_scc0 7f\n"
    "v_pk_mul_f32 v[168:169], v[168:169], %14 op_sel_hi:[1,0]\n"
    "v_pk_mul_f32 v[170:171], v[170:171], %14 op_sel_hi:[1,0]\n"
    "v_pk_mul_f32 v[172:173], v[172:173], %14 op_sel_hi:[1,0]\n"
    "v_pk_mul_f32 v[174:175], v[174:175], %14 op_sel_hi:[1,0]\n"
    "v_pk_mul_f32 v[176:177], v[176:177], %14 op_sel_hi:[1,0]\n"
    "v_pk_mul_f32 v[178:179], v[178:179], %14 op_sel_hi:[1,0]\n"
    "v_pk_mul_f32 v[180:181], v[180:181], %14 op_sel_hi:[1,0]\n"
    "v_pk_mul_f32 v[182:183], v[182:183], %14 op_sel_hi:[1,0]\n"
    "v_pk_mul_f32 v[184:185], v[184:185], %14 op_sel_hi:[1,0]\n"
    "v_pk_mul_f32 v[186:187], v[186:187], %14 op_sel_hi:[1,0]\n"
    "v_pk_mul_f32 v[188:189], v[188:189], %14 op_sel_hi:[1,0]\n"
    "v_pk_mul_f32 v[190:191], v[190:191], %14 op_sel_hi:[1,0]\n"
    "v_pk_mul_f32 v[192:193], v[192:193], %14 op_sel_hi:[1,0]\n"
    "v_pk_mul_f32 v[194:195], v[194:195], %14 op_sel_hi:[1,0]\n"
    "v_pk_mul_f32 v[196:197], v[196:197], %14 op_sel_hi:[1,0]\n"
    "v_pk_mul_f32 v[198:199], v[198:199], %14 op_sel_hi:[1,0]\n"
    "v_pk_mul_f32 v[200:201], v[200:201], %14 op_sel_hi:[1,0]\n"
    "v_pk_mul_f32 v[202:203], v[202:203], %14 op_sel_hi:[1,0]\n"
    "v_pk_mul_f32 v[204:205], v[204:205], %14 op_sel_hi:[1,0]\n"
    "v_pk_mul_f32 v[206:207], v[206:207], %14 op_sel_hi:[1,0]\n"
    "7:\n"
    "v_cndmask_b32 v120, v68, v64, vcc\n"
    "v_cndmask_b32 v121, v69, v65, vcc\n"
    "v_cndmask_b32 v122, v70, v66, vcc\n"
    "v_cndmask_b32 v123, v71, v67, vcc\n"
    "v_cvt_pk_bf16_f32 v100, v168, v169\n"
    "v_cvt_pk_bf16_f32 v101, v170, v171\n"
    "v_cvt_pk_bf16_f32 v102, v176, v177\n"
    "v_cvt_pk_bf16_f32 v103, v178, v179\n"
    "v_cvt_pk_bf16_f32 v104, v184, v185\n"
    "v_cvt_pk_bf16_f32 v105, v186, v187\n"
    "v_cvt_pk_bf16_f32 v106, v192, v193\n"
    "v_cvt_pk_bf16_f32 v107, v194, v195\n"
    "v_cvt_pk_bf16_f32 v108, v200, v201\n"
    "v_cvt_pk_bf16_f32 v109, v202, v203\n"
    "ds_write_b128 v120, v[100:103] offset:20992\n"
    "ds_write_b128 v120, v[104:107] offset:21504\n"
    "ds_write_b64 v122, v[108:109] offset:20992\n"
    "v_cvt_pk_bf16_f32 v110, v172, v173\n"
    "v_cvt_pk_bf16_f32 v111, v174, v175\n"
    "v_cvt_pk_bf16_f32 v112, v180, v181\n"
    "v_cvt_pk_bf16_f32 v113, v182, v183\n"
    "v_cvt_pk_bf16_f32 v114, v188, v189\n"
    "v_cvt_pk_bf16_f32 v115, v190, v191\n"
    "v_cvt_pk_bf16_f32 v116, v196, v197\n"
    "v_cvt_pk_bf16_f32 v117, v198, v199\n"
    "v_cvt_pk_bf16_f32 v118, v204, v205\n"
    "v_cvt_pk_bf16_f32 v119, v206, v207\n"
    "ds_write_b128 v121, v[110:113] offset:20992\n"
    "ds_write_b128 v121, v[114:117] offset:21504\n"
    "ds_write_b64 v123, v[118:119] offset:20992\n"
    "s_waitcnt lgkmcnt(0)\n"
    "s_barrier\n"
    "s_cmp_lt_u32 %17, 6\n"
    "s_cselect_b64 vcc, -1, 0\n"
    "s_cbranch_scc0 8f\n"
    "v_pk_mul_f32 v[208:209], v[208:209], %14 op_sel_hi:[1,0]\n"
    "v_pk_mul_f32 v[210:211], v[210:211], %14 op_sel_hi:[1,0]\n"
    "v_pk_mul_f32 v[212:213], v[212:213], %14 op_sel_hi:[1,0]\n"
    "v_pk_mul_f32 v[214:215], v[214:215], %14 op_sel_hi:[1,0]\n"
    "v_pk_mul_f32 v[216:217], v[216:217], %14 op_sel_hi:[1,0]\n"
    "v_pk_mul_f32 v[218:219], v[218:219], %14 op_sel_hi:[1,0]\n"
    "v_pk_mul_f32 v[220:221], v[220:221], %14 op_sel_hi:[1,0]\n"
    "v_pk_mul_f32 v[222:223], v[222:223], %14 op_sel_hi:[1,0]\n"
    "v_pk_mul_f32 v[224:225], v[224:225], %14 op_sel_hi:[1,0]\n"
    "v_pk_mul_f32 v[226:227], v[226:227], %14 op_sel_hi:[1,0]\n"
    "v_pk_mul_f32 v[228:229], v[228:229], %14 op_sel_hi:[1,0]\n"
    "v_pk_mul_f32 v[230:231], v[230:231], %14 op_sel_hi:[1,0]\n"
    "v_pk_mul_f32 v[232:233], v[232:233], %14 op_sel_hi:[1,0]\n"
    "v_pk_mul_f32 v[234:235], v[234:235], %14 op_sel_hi:[1,0]\n"
    "v_pk_mul_f32 v[236:237], v[236:237], %14 op_sel_hi:[1,0]\n"
    "v_pk_mul_f32 v[238:239], v[238:239], %14 op_sel_hi:[1,0]\n"
    "v_pk_mul_f32 v[240:241], v[240:241], %14 op_sel_hi:[1,0]\n"
    "v_pk_mul_f32 v[242:243], v[242:243], %14 op_sel_hi:[1,0]\n"
    "v_pk_mul_f32 v[244:245], v[244:245], %14 op_sel_hi:[1,0]\n"
    "v_pk_mul_f32 v[246:247], v[246:247], %14 op_sel_hi:[1,0]\n"
    "8:\n"
    "v_cndmask_b32 v120, v68, v64, vcc\n"
    "v_cndmask_b32 v121, v69, v65, vcc\n"
    "v_cndmask_b32 v122, v70, v66, vcc\n"
    "v_cndmask_b32 v123, v71, v67, vcc\n"
    "v_cvt_pk_bf16_f32 v80, v208, v209\n"
    "v_cvt_pk_bf16_f32 v81, v210, v211\n"
    "v_cvt_pk_bf16_f32 v82, v216, v217\n"
    "v_cvt_pk_bf16_f32 v83, v218, v219\n"
    "v_cvt_pk_bf16_f32 v84, v224, v225\n"
    "v_cvt_pk_bf16_f32 v85, v226, v227\n"
    "v_cvt_pk_bf16_f32 v86, v232, v233\n"
    "v_cvt_pk_bf16_f32 v87, v234, v235\n"
    "v_cvt_pk_bf16_f32 v88, v240, v241\n"
    "v_cvt_pk_bf16_f32 v89, v242, v243\n"
    "ds_write_b128 v120, v[80:83] offset:41984\n"
    "ds_write_b128 v120, v[84:87] offset:42496\n"
    "ds_write_b64 v122, v[88:89] offset:41984\n"
    "v_cvt_pk_bf16_f32 v90, v212, v213\n"
    "v_cvt_pk_bf16_f32 v91, v214, v215\n"
    "v_cvt_pk_bf16_f32 v92, v220, v221\n"
    "v_cvt_pk_bf16_f32 v93, v222, v223\n"
    "v_cvt_pk_bf16_f32 v94, v228, v229\n"
    "v_cvt_pk_bf16_f32 v95, v230, v231\n"
    "v_cvt_pk_bf16_f32 v96, v236, v237\n"
    "v_cvt_pk_bf16_f32 v97, v238, v239\n"
    "v_cvt_pk_bf16_f32 v98, v244, v245\n"
    "v_cvt_pk_bf16_f32 v99, v246, v247\n"
    "ds_write_b128 v121, v[90:93] offset:41984\n"
    "ds_write_b128 v121, v[94:97] offset:42496\n"
    "ds_write_b64 v123, v[98:99] offset:41984\n"
    "s_waitcnt lgkmcnt(0)\n"
    "s_barrier\n"
    "5:\n");
        else if (j == 5) K1_LOOP(
    "v_mov_b32 v120, 0\n"
    "global_load_dwordx2 %5, v120, %19 sc1\n"
    "v_mov_b64 v[128:129], 0\n"
    "v_mov_b64 v[130:131], 0\n"
    "v_mov_b64 v[132:133], 0\n"
    "v_mov_b64 v[134:135], 0\n"
    "v_mov_b64 v[136:137], 0\n"
    "v_mov_b64 v[138:139], 0\n"
    "v_mov_b64 v[140:141], 0\n"
    "v_mov_b64 v[142:143], 0\n"
    "v_mov_b64 v[144:145], 0\n"
    "v_mov_b64 v[146:147], 0\n"
    "v_mov_b64 v[148:149], 0\n"
    "v_mov_b64 v[150:151], 0\n"
    "v_mov_b64 v[152:153], 0\n"
    "v_mov_b64 v[154:155], 0\n"
    "v_mov_b64 v[156:157], 0\n"
    "v_mov_b64 v[158:159], 0\n"
    "v_mov_b64 v[160:161], 0\n"
    "v_mov_b64 v[162:163], 0\n"
    "v_mov_b64 v[164:165], 0\n"
    "v_mov_b64 v[166:167], 0\n"
    "v_mov_b64 v[168:169], 0\n"
    "v_mov_b64 v[170:171], 0\n"
    "v_mov_b64 v[172:173], 0\n"
    "v_mov_b64 v[174:175], 0\n"
    "v_mov_b64 v[176:177], 0\n"
    "v_mov_b64 v[178:179], 0\n"
    "v_mov_b64 v[180:181], 0\n"
    "v_mov_b64 v[182:183], 0\n"
    "v_mov_b64 v[184:185], 0\n"
    "v_mov_b64 v[186:187], 0\n"
    "v_mov_b64 v[188:189], 0\n"
    "v_mov_b64 v[190:191], 0\n"
    "v_mov_b64 v[192:193], 0\n"
    "v_mov_b64 v[194:195], 0\n"
    "v_mov_b64 v[196:197], 0\n"
    "v_mov_b64 v[198:199], 0\n"
    "v_mov_b64 v[200:201], 0\n"
    "v_mov_b64 v[202:203], 0\n"
    "v_mov_b64 v[204:205], 0\n"
    "v_mov_b64 v[206:207], 0\n"
    "v_mov_b64 v[208:209], 0\n"
    "v_mov_b64 v[210:211], 0\n"
    "v_mov_b64 v[212:213], 0\n"
    "v_mov_b64 v[214:215], 0\n"
    "v_mov_b64 v[216:217], 0\n"
    "v_mov_b64 v[218:219], 0\n"
    "v_mov_b64 v[220:221], 0\n"
    "v_mov_b64 v[222:223], 0\n"
    "v_mov_b64 v[224:225], 0\n"
    "v_mov_b64 v[226:227], 0\n"
    "v_mov_b64 v[228:229], 0\n"
    "v_mov_b64 v[230:231], 0\n"
    "v_mov_b64 v[232:233], 0\n"
    "v_mov_b64 v[234:235], 0\n"
    "v_mov_b64 v[236:237], 0\n"
    "v_mov_b64 v[238:239], 0\n"
    "v_mov_b64 v[240:241], 0\n"
    "v_mov_b64 v[242:243], 0\n"
    "v_mov_b64 v[244:245], 0\n"
    "v_mov_b64 v[246:247], 0\n"
    "s_waitcnt lgkmcnt(0)\n"
    "s_barrier\n"
    "s_mov_b32 %10, 0\n"
    "s_mov_b32 %11, 0\n"
    "ds_read_b128 v[80:83], %8\n"
    "ds_read_b128 v[84:87], %8 offset:2048\n"
    "ds_read_b64_tr_b16 v[96:97], %6 offset:0\n"
    "ds_read_b64_tr_b16 v[98:99], %6 offset:7680\n"
    "ds_read_b64_tr_b16 v[100:101], %6 offset:8\n"
    "ds_read_b64_tr_b16 v[102:103], %6 offset:7688\n"
    "ds_read_b64_tr_b16 v[104:105], %6 offset:16\n"
    "ds_read_b64_tr_b16 v[106:107], %6 offset:7696\n"
    "ds_read_b64_tr_b16 v[108:109], %6 offset:24\n"
    "ds_read_b64_tr_b16 v[110:111], %6 offset:7704\n"
    "ds_read_b64_tr_b16 v[112:113], %6 offset:32\n"
    "ds_read_b64_tr_b16 v[114:115], %6 offset:7712\n"
    "s_waitcnt lgkmcnt(8)\n"
    ".p2align 6\n"
    "1:\n"
    "v_mfma_f32_16x16x32_bf16 v[128:131], v[96:99], v[80:83], v[128:131]\n"
    "ds_read_b64_tr_b16 v[116:117], %7 offset:160\n"
    "v_mfma_f32_16x16x32_bf16 v[132:135], v[96:99], v[84:87], v[132:135]\n"
    "s_waitcnt lgkmcnt(6)\n"
    "ds_read_b64_tr_b16 v[118:119], %7 offset:7840\n"
    "v_mfma_f32_16x16x32_bf16 v[136:139], v[100:103], v[80:83], v[136:139]\n"
    "ds_read_b64_tr_b16 v[96:97], %7 offset:192\n"
    "v_mfma_f32_16x16x32_bf16 v[140:143], v[100:103], v[84:87], v[140:143]\n"
    "s_waitcnt lgkmcnt(7)\n"
    "ds_read_b64_tr_b16 v[98:99], %7 offset:7872\n"
    "v_mfma_f32_16x16x32_bf16 v[144:147], v[104:107], v[80:83], v[144:147]\n"
    "s_add_u32 %11, %11, 1\n"
    "ds_read_b64_tr_b16 v[100:101], %7 offset:224\n"
    "v_mfma_f32_16x16x32_bf16 v[148:151], v[104:107], v[84:87], v[148:151]\n"
    "s_waitcnt lgkmcnt(7)\n"
    "ds_read_b64_tr_b16 v[102:103], %7 offset:7904\n"
    "v_mfma_f32_16x16x32_bf16 v[152:155], v[108:111], v[80:83], v[152:155]\n"
    "s_mov_b32 %12, 15360\n"
    "ds_read_b64_tr_b16 v[104:105], %7 offset:256\n"
    "v_mfma_f32_16x16x32_bf16 v[156:159], v[108:111], v[84:87], v[156:159]\n"
    "s_waitcnt lgkmcnt(7)\n"
    "ds_read_b64_tr_b16 v[106:107], %7 offset:7936\n"
    "v_mfma_f32_16x16x32_bf16 v[160:163], v[112:115], v[80:83], v[160:163]\n"
    "s_cmp_eq_u32 %11, 7\n"
    "ds_read_b64_tr_b16 v[108:109], %7 offset:288\n"
    "v_mfma_f32_16x16x32_bf16 v[164:167], v[112:115], v[84:87], v[164:167]\n"
    "s_waitcnt lgkmcnt(7)\n"
    "ds_read_b64_tr_b16 v[110:111], %7 offset:7968\n"
    "v_mfma_f32_16x16x32_bf16 v[168:171], v[80:83], v[116:119], v[168:171]\n"
    "s_cmov_b32 %12, -92160\n"
    "ds_read_b64_tr_b16 v[112:113], %7 offset:320\n"
    "v_mfma_f32_16x16x32_bf16 v[172:175], v[84:87], v[116:119], v[172:175]\n"
    "s_waitcnt lgkmcnt(7)\n"
    "ds_read_b64_tr_b16 v[114:115], %7 offset:8000\n"
    "v_mfma_f32_16x16x32_bf16 v[176:179], v[80:83], v[96:99], v[176:179]\n"
    "s_cmov_b32 %11, 0\n"
    "ds_read_b64_tr_b16 v[116:117], %7 offset:352\n"
    "v_mfma_f32_16x16x32_bf16 v[180:183], v[84:87], v[96:99], v[180:183]\n"
    "s_waitcnt lgkmcnt(7)\n"
    "ds_read_b64_tr_b16 v[118:119], %7 offset:8032\n"
    "v_mfma_f32_16x16x32_bf16 v[184:187], v[80:83], v[100:103], v[184:187]\n"
    "ds_read_b64_tr_b16 v[96:97], %7 offset:384\n"
    "v_mfma_f32_16x16x32_bf16 v[188:191], v[84:87], v[100:103], v[188:191]\n"
    "s_waitcnt lgkmcnt(7)\n"
    "ds_read_b64_tr_b16 v[98:99], %7 offset:8064\n"
    "v_mfma_f32_16x16x32_bf16 v[192:195], v[80:83], v[104:107], v[192:195]\n"
    "ds_read_b64_tr_b16 v[100:101], %7 offset:416\n"
    "v_mfma_f32_16x16x32_bf16 v[196:199], v[84:87], v[104:107], v[196:199]\n"
    "s_waitcnt lgkmcnt(7)\n"
    "ds_read_b64_tr_b16 v[102:103], %7 offset:8096\n"
    "v_mfma_f32_16x16x32_bf16 v[200:203], v[80:83], v[108:111], v[200:203]\n"
    "ds_read_b64_tr_b16 v[104:105], %7 offset:448\n"
    "v_mfma_f32_16x16x32_bf16 v[204:207], v[84:87], v[108:111], v[204:207]\n"
    "s_waitcnt lgkmcnt(7)\n"
    "ds_read_b64_tr_b16 v[106:107], %7 offset:8128\n"
    "v_add_u32 %6, %12, %6\n"
    "v_add_u32 %7, %12, %7\n"
    "v_mfma_f32_16x16x32_bf16 v[208:211], v[80:83], v[112:115], v[208:211]\n"
    "ds_read_b64_tr_b16 v[108:109], %6 offset:0\n"
    "v_mfma_f32_16x16x32_bf16 v[212:215], v[84:87], v[112:115], v[212:215]\n"
    "s_waitcnt lgkmcnt(7)\n"
    "ds_read_b64_tr_b16 v[110:111], %6 offset:7680\n"
    "v_mfma_f32_16x16x32_bf16 v[216:219], v[80:83], v[116:119], v[216:219]\n"
    "ds_read_b64_tr_b16 v[112:113], %6 offset:8\n"
    "ds_read_b128 v[88:91], %9\n"
    "v_mfma_f32_16x16x32_bf16 v[220:223], v[84:87], v[116:119], v[220:223]\n"
    "s_waitcnt lgkmcnt(8)\n"
    "ds_read_b64_tr_b16 v[114:115], %6 offset:7688\n"
    "v_mfma_f32_16x16x32_bf16 v[224:227], v[80:83], v[96:99], v[224:227]\n"
    "s_add_u32 %10, %10, 1\n"
    "ds_read_b64_tr_b16 v[116:117], %6 offset:16\n"
    "ds_read_b128 v[92:95], %9 offset:2048\n"
    "v_mfma_f32_16x16x32_bf16 v[228:231], v[84:87], v[96:99], v[228:231]\n"
    "s_waitcnt lgkmcnt(9)\n"
    "ds_read_b64_tr_b16 v[118:119], %6 offset:7696\n"
    "v_mfma_f32_16x16x32_bf16 v[232:235], v[80:83], v[100:103], v[232:235]\n"
    "s_mul_i32 %13, %10, 11\n"
    "ds_read_b64_tr_b16 v[96:97], %6 offset:24\n"
    "v_mfma_f32_16x16x32_bf16 v[236:239], v[84:87], v[100:103], v[236:239]\n"
    "s_waitcnt lgkmcnt(9)\n"
    "ds_read_b64_tr_b16 v[98:99], %6 offset:7704\n"
    "v_mfma_f32_16x16x32_bf16 v[240:243], v[80:83], v[104:107], v[240:243]\n"
    "s_and_b32 %13, %13, 28\n"
    "ds_read_b64_tr_b16 v[100:101], %6 offset:32\n"
    "v_mfma_f32_16x16x32_bf16 v[244:247], v[84:87], v[104:107], v[244:247]\n"
    "s_waitcnt lgkmcnt(4)\n"
    "ds_read_b64_tr_b16 v[102:103], %6 offset:7712\n"
    "v_mfma_f32_16x16x32_bf16 v[128:131], v[108:111], v[88:91], v[128:131]\n"
    "s_cselect_b32 %13, %15, %16\n"
    "ds_read_b64_tr_b16 v[104:105], %7 offset:160\n"
    "v_mfma_f32_16x16x32_bf16 v[132:135], v[108:111], v[92:95], v[132:135]\n"
    "s_waitcnt lgkmcnt(6)\n"
    "ds_read_b64_tr_b16 v[106:107], %7 offset:7840\n"
    "v_mfma_f32_16x16x32_bf16 v[136:139], v[112:115], v[88:91], v[136:139]\n"
    "ds_read_b64_tr_b16 v[108:109], %7 offset:192\n"
    "v_mfma_f32_16x16x32_bf16 v[140:143], v[112:115], v[92:95], v[140:143]\n"
    "s_waitcnt lgkmcnt(7)\n"
    "ds_read_b64_tr_b16 v[110:111], %7 offset:7872\n"
    "v_mfma_f32_16x16x32_bf16 v[144:147], v[116:119], v[88:91], v[144:147]\n"
    "s_add_u32 %11, %11, 1\n"
    "ds_read_b64_tr_b16 v[112:113], %7 offset:224\n"
    "v_mfma_f32_16x16x32_bf16 v[148:151], v[116:119], v[92:95], v[148:151]\n"
    "s_waitcnt lgkmcnt(7)\n"
    "ds_read_b64_tr_b16 v[114:115], %7 offset:7904\n"
    "v_mfma_f32_16x16x32_bf16 v[152:155], v[96:99], v[88:91], v[152:155]\n"
    "s_mov_b32 %12, 15360\n"
    "ds_read_b64_tr_b16 v[116:117], %7 offset:256\n"
    "v_mfma_f32_16x16x32_bf16 v[156:159], v[96:99], v[92:95], v[156:159]\n"
    "s_waitcnt lgkmcnt(7)\n"
    "ds_read_b64_tr_b16 v[118:119], %7 offset:7936\n"
    "v_mfma_f32_16x16x32_bf16 v[160:163], v[100:103], v[88:91], v[160:163]\n"
    "s_cmp_eq_u32 %11, 7\n"
    "ds_read_b64_tr_b16 v[96:97], %7 offset:288\n"
    "v_mfma_f32_16x16x32_bf16 v[164:167], v[100:103], v[92:95], v[164:167]\n"
    "s_waitcnt lgkmcnt(7)\n"
    "ds_read_b64_tr_b16 v[98:99], %7 offset:7968\n"
    "v_mfma_f32_16x16x32_bf16 v[168:171], v[88:91], v[104:107], v[168:171]\n"
    "s_cmov_b32 %12, -92160\n"
    "ds_read_b64_tr_b16 v[100:101], %7 offset:320\n"
    "v_mfma_f32_16x16x32_bf16 v[172:175], v[92:95], v[104:107], v[172:175]\n"
    "s_waitcnt lgkmcnt(7)\n"
    "ds_read_b64_tr_b16 v[102:103], %7 offset:8000\n"
    "v_mfma_f32_16x16x32_bf16 v[176:179], v[88:91], v[108:111], v[176:179]\n"
    "s_cmov_b32 %11, 0\n"
    "ds_read_b64_tr_b16 v[104:105], %7 offset:352\n"
    "v_mfma_f32_16x16x32_bf16 v[180:183], v[92:95], v[108:111], v[180:183]\n"
    "s_waitcnt lgkmcnt(7)\n"
    "ds_read_b64_tr_b16 v[106:107], %7 offset:8032\n"
    "v_mfma_f32_16x16x32_bf16 v[184:187], v[88:91], v[112:115], v[184:187]\n"
    "ds_read_b64_tr_b16 v[108:109], %7 offset:384\n"
    "v_mfma_f32_16x16x32_bf16 v[188:191], v[92:95], v[112:115], v[188:191]\n"
    "s_waitcnt lgkmcnt(7)\n"
    "ds_read_b64_tr_b16 v[110:111], %7 offset:8064\n"
    "v_mfma_f32_16x16x32_bf16 v[192:195], v[88:91], v[116:119], v[192:195]\n"
    "ds_read_b64_tr_b16 v[112:113], %7 offset:416\n"
    "v_mfma_f32_16x16x32_bf16 v[196:199], v[92:95], v[116:119], v[196:199]\n"
    "s_waitcnt lgkmcnt(7)\n"
    "ds_read_b64_tr_b16 v[114:115], %7 offset:8096\n"
    "v_add_u32 %8, %13, %8\n"
    "v_mfma_f32_16x16x32_bf16 v[200:203], v[88:91], v[96:99], v[200:203]\n"
    "ds_read_b64_tr_b16 v[116:117], %7 offset:448\n"
    "v_mfma_f32_16x16x32_bf16 v[204:207], v[92:95], v[96:99], v[204:207]\n"
    "s_waitcnt lgkmcnt(7)\n"
    "ds_read_b64_tr_b16 v[118:119], %7 offset:8128\n"
    "v_add_u32 %6, %12, %6\n"
    "v_add_u32 %7, %12, %7\n"
    "v_mfma_f32_16x16x32_bf16 v[208:211], v[88:91], v[100:103], v[208:211]\n"
    "s_barrier\n"
    "ds_read_b64_tr_b16 v[96:97], %6 offset:0\n"
    "v_mfma_f32_16x16x32_bf16 v[212:215], v[92:95], v[100:103], v[212:215]\n"
    "s_waitcnt lgkmcnt(7)\n"
    "ds_read_b64_tr_b16 v[98:99], %6 offset:7680\n"
    "v_mfma_f32_16x16x32_bf16 v[216:219], v[88:91], v[104:107], v[216:219]\n"
    "ds_read_b64_tr_b16 v[100:101], %6 offset:8\n"
    "ds_read_b128 v[80:83], %8\n"
    "v_mfma_f32_16x16x32_bf16 v[220:223], v[92:95], v[104:107], v[220:223]\n"
    "s_waitcnt lgkmcnt(8)\n"
    "ds_read_b64_tr_b16 v[102:103], %6 offset:7688\n"
    "v_mfma_f32_16x16x32_bf16 v[224:227], v[88:91], v[108:111], v[224:227]\n"
    "ds_read_b64_tr_b16 v[104:105], %6 offset:16\n"
    "ds_read_b128 v[84:87], %8 offset:2048\n"
    "v_mfma_f32_16x16x32_bf16 v[228:231], v[92:95], v[108:111], v[228:231]\n"
    "s_waitcnt lgkmcnt(9)\n"
    "ds_read_b64_tr_b16 v[106:107], %6 offset:7696\n"
    "v_add_u32 %9, %13, %9\n"
    "v_mfma_f32_16x16x32_bf16 v[232:235], v[88:91], v[112:115], v[232:235]\n"
    "s_cmp_lt_u32 %10, 10\n"
    "ds_read_b64_tr_b16 v[108:109], %6 offset:24\n"
    "v_mfma_f32_16x16x32_bf16 v[236:239], v[92:95], v[112:115], v[236:239]\n"
    "s_waitcnt lgkmcnt(9)\n"
    "ds_read_b64_tr_b16 v[110:111], %6 offset:7704\n"
    "v_mfma_f32_16x16x32_bf16 v[240:243], v[88:91], v[116:119], v[240:243]\n"
    "ds_read_b64_tr_b16 v[112:113], %6 offset:32\n"
    "v_mfma_f32_16x16x32_bf16 v[244:247], v[92:95], v[116:119], v[244:247]\n"
    "s_waitcnt lgkmcnt(4)\n"
    "ds_read_b64_tr_b16 v[114:115], %6 offset:7712\n"
    "s_cbranch_scc1 1b\n"
    "s_waitcnt vmcnt(0) lgkmcnt(0)\n"
    "s_nop 7\n"
    "v_mov_b32 v124, 0x3f803f80\n"
    "v_mov_b32 v125, 0x3f803f80\n"
    "v_mov_b32 v126, 0x3f803f80\n"
    "v_mov_b32 v127, 0x3f803f80\n"
    "v_cvt_pk_bf16_f32 v80, v128, v129\n"
    "v_cvt_pk_bf16_f32 v81, v130, v131\n"
    "v_cvt_pk_bf16_f32 v82, v136, v137\n"
    "v_cvt_pk_bf16_f32 v83, v138, v139\n"
    "v_cvt_pk_bf16_f32 v84, v144, v145\n"
    "v_cvt_pk_bf16_f32 v85, v146, v147\n"
    "v_cvt_pk_bf16_f32 v86, v152, v153\n"
    "v_cvt_pk_bf16_f32 v87, v154, v155\n"
    "v_cvt_pk_bf16_f32 v88, v160, v161\n"
    "v_cvt_pk_bf16_f32 v89, v162, v163\n"
    "ds_write_b128 v68, v[80:83] offset:0\n"
    "ds_write_b128 v68, v[84:87] offset:512\n"
    "ds_write_b64 v70, v[88:89] offset:0\n"
    "v_cvt_pk_bf16_f32 v90, v132, v133\n"
    "v_cvt_pk_bf16_f32 v91, v134, v135\n"
    "v_cvt_pk_bf16_f32 v92, v140, v141\n"
    "v_cvt_pk_bf16_f32 v93, v142, v143\n"
    "v_cvt_pk_bf16_f32 v94, v148, v149\n"
    "v_cvt_pk_bf16_f32 v95, v150, v151\n"
    "v_cvt_pk_bf16_f32 v96, v156, v157\n"
    "v_cvt_pk_bf16_f32 v97, v158, v159\n"
    "v_cvt_pk_bf16_f32 v98, v164, v165\n"
    "v_cvt_pk_bf16_f32 v99, v166, v167\n"
    "ds_write_b128 v69, v[90:93] offset:0\n"
    "ds_write_b128 v69, v[94:97] offset:512\n"
    "ds_write_b64 v71, v[98:99] offset:0\n"
    "s_waitcnt lgkmcnt(0)\n"
    "s_barrier\n"
    "v_cvt_pk_bf16_f32 v100, v168, v169\n"
    "v_cvt_pk_bf16_f32 v101, v170, v171\n"
    "v_cvt_pk_bf16_f32 v102, v172, v173\n"
    "v_cvt_pk_bf16_f32 v103, v174, v175\n"
    "ds_write_b128 v72, v[100:103] offset:20992\n"
    "v_cvt_pk_bf16_f32 v104, v176, v177\n"
    "v_cvt_pk_bf16_f32 v105, v178, v179\n"
    "v_cvt_pk_bf16_f32 v106, v180, v181\n"
    "v_cvt_pk_bf16_f32 v107, v182, v183\n"
    "ds_write_b128 v73, v[104:107] offset:20992\n"
    "v_cvt_pk_bf16_f32 v108, v184, v185\n"
    "v_cvt_pk_bf16_f32 v109, v186, v187\n"
    "v_cvt_pk_bf16_f32 v110, v188, v189\n"
    "v_cvt_pk_bf16_f32 v111, v190, v191\n"
    "ds_write_b128 v74, v[108:111] offset:20992\n"
    "v_cvt_pk_bf16_f32 v112, v192, v193\n"
    "v_cvt_pk_bf16_f32 v113, v194, v195\n"
    "v_cvt_pk_bf16_f32 v114, v196, v197\n"
    "v_cvt_pk_bf16_f32 v115, v198, v199\n"
    "ds_write_b128 v75, v[112:115] offset:20992\n"
    "v_cvt_pk_bf16_f32 v116, v200, v201\n"
    "v_cvt_pk_bf16_f32 v117, v202, v203\n"
    "v_cvt_pk_bf16_f32 v118, v204, v205\n"
    "v_cvt_pk_bf16_f32 v119, v206, v207\n"
    "ds_write_b128 v76, v[116:119] offset:20992\n"
    "s_mov_b32 exec_lo, 15\n"
    "s_mov_b32 exec_hi, 15\n"
    "ds_write_b128 v77, v[124:127] offset:20992\n"
    "s_mov_b64 exec, -1\n"
    "s_waitcnt lgkmcnt(0)\n"
    "s_barrier\n"
    "v_cvt_pk_bf16_f32 v80, v208, v209\n"
    "v_cvt_pk_bf16_f32 v81, v210, v211\n"
    "v_cvt_pk_bf16_f32 v82, v212, v213\n"
    "v_cvt_pk_bf16_f32 v83, v214, v215\n"
    "ds_write_b128 v72, v[80:83] offset:41984\n"
    "v_cvt_pk_bf16_f32 v84, v216, v217\n"
    "v_cvt_pk_bf16_f32 v85, v218, v219\n"
    "v_cvt_pk_bf16_f32 v86, v220, v221\n"
    "v_cvt_pk_bf16_f32 v87, v222, v223\n"
    "ds_write_b128 v73, v[84:87] offset:41984\n"
    "v_cvt_pk_bf16_f32 v88, v224, v225\n"
    "v_cvt_pk_bf16_f32 v89, v226, v227\n"
    "v_cvt_pk_bf16_f32 v90, v228, v229\n"
    "v_cvt_pk_bf16_f32 v91, v230, v231\n"
    "ds_write_b128 v74, v[88:91] offset:41984\n"
    "v_cvt_pk_bf16_f32 v92, v232, v233\n"
    "v_cvt_pk_bf16_f32 v93, v234, v235\n"
    "v_cvt_pk_bf16_f32 v94, v236, v237\n"
    "v_cvt_pk_bf16_f32 v95, v238, v239\n"
    "ds_write_b128 v75, v[92:95] offset:41984\n"
    "v_cvt_pk_bf16_f32 v96, v240, v241\n"
    "v_cvt_pk_bf16_f32 v97, v242, v243\n"
    "v_cvt_pk_bf16_f32 v98, v244, v245\n"
    "v_cvt_pk_bf16_f32 v99, v246, v247\n"
    "ds_write_b128 v76, v[96:99] offset:41984\n"
    "s_mov_b32 exec_lo, 15\n"
    "s_mov_b32 exec_hi, 15\n"
    "ds_write_b128 v77, v[124:127] offset:41984\n"
    "s_mov_b64 exec, -1\n"
    "s_waitcnt lgkmcnt(0)\n"
    "s_barrier\n");
        else K1_LOOP(
    "v_mov_b32 v120, 0\n"
    "global_load_dwordx2 %5, v120, %19 sc1\n"
    "v_mov_b64 v[128:129], 0\n"
    "v_mov_b64 v[130:131], 0\n"
    "v_mov_b64 v[132:133], 0\n"
    "v_mov_b64 v[134:135], 0\n"
    "v_mov_b64 v[136:137], 0\n"
    "v_mov_b64 v[138:139], 0\n"
    "v_mov_b64 v[140:141], 0\n"
    "v_mov_b64 v[142:143], 0\n"
    "v_mov_b64 v[144:145], 0\n"
    "v_mov_b64 v[146:147], 0\n"
    "v_mov_b64 v[148:149], 0\n"
    "v_mov_b64 v[150:151], 0\n"
    "v_mov_b64 v[152:153], 0\n"
    "v_mov_b64 v[154:155], 0\n"
    "v_mov_b64 v[156:157], 0\n"
    "v_mov_b64 v[158:159], 0\n"
    "v_mov_b64 v[160:161], 0\n"
    "v_mov_b64 v[162:163], 0\n"
    "v_mov_b64 v[164:165], 0\n"
    "v_mov_b64 v[166:167], 0\n"
    "v_mov_b64 v[168:169], 0\n"
    "v_mov_b64 v[170:171], 0\n"
    "v_mov_b64 v[172:173], 0\n"
    "v_mov_b64 v[174:175], 0\n"
    "v_mov_b64 v[176:177], 0\n"
    "v_mov_b64 v[178:179], 0\n"
    "v_mov_b64 v[180:181], 0\n"
    "v_mov_b64 v[182:183], 0\n"
    "v_mov_b64 v[184:185], 0\n"
    "v_mov_b64 v[186:187], 0\n"
    "v_mov_b64 v[188:189], 0\n"
    "v_mov_b64 v[190:191], 0\n"
    "v_mov_b64 v[192:193], 0\n"
    "v_mov_b64 v[194:195], 0\n"
    "v_mov_b64 v[196:197], 0\n"
    "v_mov_b64 v[198:199], 0\n"
    "v_mov_b64 v[200:201], 0\n"
    "v_mov_b64 v[202:203], 0\n"
    "v_mov_b64 v[204:205], 0\n"
    "v_mov_b64 v[206:207], 0\n"
    "v_mov_b64 v[208:209], 0\n"
    "v_mov_b64 v[210:211], 0\n"
    "v_mov_b64 v[212:213], 0\n"
    "v_mov_b64 v[214:215], 0\n"
    "v_mov_b64 v[216:217], 0\n"
    "v_mov_b64 v[218:219], 0\n"
    "v_mov_b64 v[220:221], 0\n"
    "v_mov_b64 v[222:223], 0\n"
    "v_mov_b64 v[224:225], 0\n"
    "v_mov_b64 v[226:227], 0\n"
    "v_mov_b64 v[228:229], 0\n"
    "v_mov_b64 v[230:231], 0\n"
    "v_mov_b64 v[232:233], 0\n"
    "v_mov_b64 v[234:235], 0\n"
    "v_mov_b64 v[236:237], 0\n"
    "v_mov_b64 v[238:239], 0\n"
    "v_mov_b64 v[240:241], 0\n"
    "v_mov_b64 v[242:243], 0\n"
    "v_mov_b64 v[244:245], 0\n"
    "v_mov_b64 v[246:247], 0\n"
    "s_waitcnt lgkmcnt(0)\n"
    "s_barrier\n"
    "s_mov_b32 %10, 0\n"
    "s_mov_b32 %11, 0\n"
    "ds_read_b128 v[80:83], %8\n"
    "ds_read_b128 v[84:87], %8 offset:2048\n"
    "ds_read_b64_tr_b16 v[96:97], %7 offset:0\n"
    "ds_read_b64_tr_b16 v[98:99], %7 offset:7680\n"
    "ds_read_b64_tr_b16 v[100:101], %7 offset:32\n"
    "ds_read_b64_tr_b16 v[102:103], %7 offset:7712\n"
    "ds_read_b64_tr_b16 v[104:105], %7 offset:64\n"
    "ds_read_b64_tr_b16 v[106:107], %7 offset:7744\n"
    "ds_read_b64_tr_b16 v[108:109], %7 offset:96\n"
    "ds_read_b64_tr_b16 v[110:111], %7 offset:7776\n"
    "ds_read_b64_tr_b16 v[112:113], %7 offset:128\n"
    "ds_read_b64_tr_b16 v[114:115], %7 offset:7808\n"
    "s_waitcnt lgkmcnt(8)\n"
    ".p2align 6\n"
    "1:\n"
    "v_mfma_f32_16x16x32_bf16 v[128:131], v[80:83], v[96:99], v[128:131]\n"
    "ds_read_b64_tr_b16 v[116:117], %7 offset:160\n"
    "v_mfma_f32_16x16x32_bf16 v[132:135], v[84:87], v[96:99], v[132:135]\n"
    "s_waitcnt lgkmcnt(6)\n"
    "ds_read_b64_tr_b16 v[118:119], %7 offset:7840\n"
    "v_mfma_f32_16x16x32_bf16 v[136:139], v[80:83], v[100:103], v[136:139]\n"
    "ds_read_b64_tr_b16 v[96:97], %7 offset:192\n"
    "v_mfma_f32_16x16x32_bf16 v[140:143], v[84:87], v[100:103], v[140:143]\n"
    "s_waitcnt lgkmcnt(7)\n"
    "ds_read_b64_tr_b16 v[98:99], %7 offset:7872\n"
    "v_mfma_f32_16x16x32_bf16 v[144:147], v[80:83], v[104:107], v[144:147]\n"
    "s_add_u32 %11, %11, 1\n"
    "ds_read_b64_tr_b16 v[100:101], %7 offset:224\n"
    "v_mfma_f32_16x16x32_bf16 v[148:151], v[84:87], v[104:107], v[148:151]\n"
    "s_waitcnt lgkmcnt(7)\n"
    "ds_read_b64_tr_b16 v[102:103], %7 offset:7904\n"
    "v_mfma_f32_16x16x32_bf16 v[152:155], v[80:83], v[108:111], v[152:155]\n"
    "s_mov_b32 %12, 15360\n"
    "ds_read_b64_tr_b16 v[104:105], %7 offset:256\n"
    "v_mfma_f32_16x16x32_bf16 v[156:159], v[84:87], v[108:111], v[156:159]\n"
    "s_waitcnt lgkmcnt(7)\n"
    "ds_read_b64_tr_b16 v[106:107], %7 offset:7936\n"
    "v_mfma_f32_16x16x32_bf16 v[160:163], v[80:83], v[112:115], v[160:163]\n"
    "s_cmp_eq_u32 %11, 7\n"
    "ds_read_b64_tr_b16 v[108:109], %7 offset:288\n"
    "v_mfma_f32_16x16x32_bf16 v[164:167], v[84:87], v[112:115], v[164:167]\n"
    "s_waitcnt lgkmcnt(7)\n"
    "ds_read_b64_tr_b16 v[110:111], %7 offset:7968\n"
    "v_mfma_f32_16x16x32_bf16 v[168:171], v[80:83], v[116:119], v[168:171]\n"
    "s_cmov_b32 %12, -92160\n"
    "ds_read_b64_tr_b16 v[112:113], %7 offset:320\n"
    "v_mfma_f32_16x16x32_bf16 v[172:175], v[84:87], v[116:119], v[172:175]\n"
    "s_waitcnt lgkmcnt(7)\n"
    "ds_read_b64_tr_b16 v[114:115], %7 offset:8000\n"
    "v_mfma_f32_16x16x32_bf16 v[176:179], v[80:83], v[96:99], v[176:179]\n"
    "s_cmov_b32 %11, 0\n"
    "ds_read_b64_tr_b16 v[116:117], %7 offset:352\n"
    "v_mfma_f32_16x16x32_bf16 v[180:183], v[84:87], v[96:99], v[180:183]\n"
    "s_waitcnt lgkmcnt(7)\n"
    "ds_read_b64_tr_b16 v[118:119], %7 offset:8032\n"
    "v_mfma_f32_16x16x32_bf16 v[184:187], v[80:83], v[100:103], v[184:187]\n"
    "ds_read_b64_tr_b16 v[96:97], %7 offset:384\n"
    "v_mfma_f32_16x16x32_bf16 v[188:191], v[84:87], v[100:103], v[188:191]\n"
    "s_waitcnt lgkmcnt(7)\n"
    "ds_read_b64_tr_b16 v[98:99], %7 offset:8064\n"
    "v_mfma_f32_16x16x32_bf16 v[192:195], v[80:83], v[104:107], v[192:195]\n"
    "ds_read_b64_tr_b16 v[100:101], %7 offset:416\n"
    "v_mfma_f32_16x16x32_bf16 v[196:199], v[84:87], v[104:107], v[196:199]\n"
    "s_waitcnt lgkmcnt(7)\n"
    "ds_read_b64_tr_b16 v[102:103], %7 offset:8096\n"
    "v_mfma_f32_16x16x32_bf16 v[200:203], v[80:83], v[108:111], v[200:203]\n"
    "ds_read_b64_tr_b16 v[104:105], %7 offset:448\n"
    "v_mfma_f32_16x16x32_bf16 v[204:207], v[84:87], v[108:111], v[204:207]\n"
    "s_waitcnt lgkmcnt(7)\n"
    "ds_read_b64_tr_b16 v[106:107], %7 offset:8128\n"
    "v_add_u32 %7, %12, %7\n"
    "v_mfma_f32_16x16x32_bf16 v[208:211], v[80:83], v[112:115], v[208:211]\n"
    "ds_read_b64_tr_b16 v[108:109], %7 offset:0\n"
    "v_mfma_f32_16x16x32_bf16 v[212:215], v[84:87], v[112:115], v[212:215]\n"
    "s_waitcnt lgkmcnt(7)\n"
    "ds_read_b64_tr_b16 v[110:111], %7 offset:7680\n"
    "v_mfma_f32_16x16x32_bf16 v[216:219], v[80:83], v[116:119], v[216:219]\n"
    "ds_read_b64_tr_b16 v[112:113], %7 offset:32\n"
    "ds_read_b128 v[88:91], %9\n"
    "v_mfma_f32_16x16x32_bf16 v[220:223], v[84:87], v[116:119], v[220:223]\n"
    "s_waitcnt lgkmcnt(8)\n"
    "ds_read_b64_tr_b16 v[114:115], %7 offset:7712\n"
    "v_mfma_f32_16x16x32_bf16 v[224:227], v[80:83], v[96:99], v[224:227]\n"
    "s_add_u32 %10, %10, 1\n"
    "ds_read_b64_tr_b16 v[116:117], %7 offset:64\n"
    "ds_read_b128 v[92:95], %9 offset:2048\n"
    "v_mfma_f32_16x16x32_bf16 v[228:231], v[84:87], v[96:99], v[228:231]\n"
    "s_waitcnt lgkmcnt(9)\n"
    "ds_read_b64_tr_b16 v[118:119], %7 offset:7744\n"
    "v_mfma_f32_16x16x32_bf16 v[232:235], v[80:83], v[100:103], v[232:235]\n"
    "s_mul_i32 %13, %10, 11\n"
    "ds_read_b64_tr_b16 v[96:97], %7 offset:96\n"
    "v_mfma_f32_16x16x32_bf16 v[236:239], v[84:87], v[100:103], v[236:239]\n"
    "s_waitcnt lgkmcnt(9)\n"
    "ds_read_b64_tr_b16 v[98:99], %7 offset:7776\n"
    "v_mfma_f32_16x16x32_bf16 v[240:243], v[80:83], v[104:107], v[240:243]\n"
    "s_and_b32 %13, %13, 28\n"
    "ds_read_b64_tr_b16 v[100:101], %7 offset:128\n"
    "v_mfma_f32_16x16x32_bf16 v[244:247], v[84:87], v[104:107], v[244:247]\n"
    "s_waitcnt lgkmcnt(4)\n"
    "ds_read_b64_tr_b16 v[102:103], %7 offset:7808\n"
    "v_mfma_f32_16x16x32_bf16 v[128:131], v[88:91], v[108:111], v[128:131]\n"
    "s_cselect_b32 %13, %15, %16\n"
    "ds_read_b64_tr_b16 v[104:105], %7 offset:160\n"
    "v_mfma_f32_16x16x32_bf16 v[132:135], v[92:95], v[108:111], v[132:135]\n"
    "s_waitcnt lgkmcnt(6)\n"
    "ds_read_b64_tr_b16 v[106:107], %7 offset:7840\n"
    "v_mfma_f32_16x16x32_bf16 v[136:139], v[88:91], v[112:115], v[136:139]\n"
    "ds_read_b64_tr_b16 v[108:109], %7 offset:192\n"
    "v_mfma_f32_16x16x32_bf16 v[140:143], v[92:95], v[112:115], v[140:143]\n"
    "s_waitcnt lgkmcnt(7)\n"
    "ds_read_b64_tr_b16 v[110:111], %7 offset:7872\n"
    "v_mfma_f32_16x16x32_bf16 v[144:147], v[88:91], v[116:119], v[144:147]\n"
    "s_add_u32 %11, %11, 1\n"
    "ds_read_b64_tr_b16 v[112:113], %7 offset:224\n"
    "v_mfma_f32_16x16x32_bf16 v[148:151], v[92:95], v[116:119], v[148:151]\n"
    "s_waitcnt lgkmcnt(7)\n"
    "ds_read_b64_tr_b16 v[114:115], %7 offset:7904\n"
    "v_mfma_f32_16x16x32_bf16 v[152:155], v[88:91], v[96:99], v[152:155]\n"
    "s_mov_b32 %12, 15360\n"
    "ds_read_b64_tr_b16 v[116:117], %7 offset:256\n"
    "v_mfma_f32_16x16x32_bf16 v[156:159], v[92:95], v[96:99], v[156:159]\n"
    "s_waitcnt lgkmcnt(7)\n"
    "ds_read_b64_tr_b16 v[118:119], %7 offset:7936\n"
    "v_mfma_f32_16x16x32_bf16 v[160:163], v[88:91], v[100:103], v[160:163]\n"
    "s_cmp_eq_u32 %11, 7\n"
    "ds_read_b64_tr_b16 v[96:97], %7 offset:288\n"
    "v_mfma_f32_16x16x32_bf16 v[164:167], v[92:95], v[100:103], v[164:167]\n"
    "s_waitcnt lgkmcnt(7)\n"
    "ds_read_b64_tr_b16 v[98:99], %7 offset:7968\n"
    "v_mfma_f32_16x16x32_bf16 v[168:171], v[88:91], v[104:107], v[168:171]\n"
    "s_cmov_b32 %12, -92160\n"
    "ds_read_b64_tr_b16 v[100:101], %7 offset:320\n"
    "v_mfma_f32_16x16x32_bf16 v[172:175], v[92:95], v[104:107], v[172:175]\n"
    "s_waitcnt lgkmcnt(7)\n"
    "ds_read_b64_tr_b16 v[102:103], %7 offset:8000\n"
    "v_mfma_f32_16x16x32_bf16 v[176:179], v[88:91], v[108:111], v[176:179]\n"
    "s_cmov_b32 %11, 0\n"
    "ds_read_b64_tr_b16 v[104:105], %7 offset:352\n"
    "v_mfma_f32_16x16x32_bf16 v[180:183], v[92:95], v[108:111], v[180:183]\n"
    "s_waitcnt lgkmcnt(7)\n"
    "ds_read_b64_tr_b16 v[106:107], %7 offset:8032\n"
    "v_mfma_f32_16x16x32_bf16 v[184:187], v[88:91], v[112:115], v[184:187]\n"
    "ds_read_b64_tr_b16 v[108:109], %7 offset:384\n"
    "v_mfma_f32_16x16x32_bf16 v[188:191], v[92:95], v[112:115], v[188:191]\n"
    "s_waitcnt lgkmcnt(7)\n"
    "ds_read_b64_tr_b16 v[110:111], %7 offset:8064\n"
    "v_mfma_f32_16x16x32_bf16 v[192:195], v[88:91], v[116:119], v[192:195]\n"
    "ds_read_b64_tr_b16 v[112:113], %7 offset:416\n"
    "v_mfma_f32_16x16x32_bf16 v[196:199], v[92:95], v[116:119], v[196:199]\n"
    "s_waitcnt lgkmcnt(7)\n"
    "ds_read_b64_tr_b16 v[114:115], %7 offset:8096\n"
    "v_add_u32 %8, %13, %8\n"
    "v_mfma_f32_16x16x32_bf16 v[200:203], v[88:91], v[96:99], v[200:203]\n"
    "ds_read_b64_tr_b16 v[116:117], %7 offset:448\n"
    "v_mfma_f32_16x16x32_bf16 v[204:207], v[92:95], v[96:99], v[204:207]\n"
    "s_waitcnt lgkmcnt(7)\n"
    "ds_read_b64_tr_b16 v[118:119], %7 offset:8128\n"
    "v_add_u32 %7, %12, %7\n"
    "v_mfma_f32_16x16x32_bf16 v[208:211], v[88:91], v[100:103], v[208:211]\n"
    "s_barrier\n"
    "ds_read_b64_tr_b16 v[96:97], %7 offset:0\n"
    "v_mfma_f32_16x16x32_bf16 v[212:215], v[92:95], v[100:103], v[212:215]\n"
    "s_waitcnt lgkmcnt(7)\n"
    "ds_read_b64_tr_b16 v[98:99], %7 offset:7680\n"
    "v_mfma_f32_16x16x32_bf16 v[216:219], v[88:91], v[104:107], v[216:219]\n"
    "ds_read_b64_tr_b16 v[100:101], %7 offset:32\n"
    "ds_read_b128 v[80:83], %8\n"
    "v_mfma_f32_16x16x32_bf16 v[220:223], v[92:95], v[104:107], v[220:223]\n"
    "s_waitcnt lgkmcnt(8)\n"
    "ds_read_b64_tr_b16 v[102:103], %7 offset:7712\n"
    "v_mfma_f32_16x16x32_bf16 v[224:227], v[88:91], v[108:111], v[224:227]\n"
    "ds_read_b64_tr_b16 v[104:105], %7 offset:64\n"
    "ds_read_b128 v[84:87], %8 offset:2048\n"
    "v_mfma_f32_16x16x32_bf16 v[228:231], v[92:95], v[108:111], v[228:231]\n"
    "s_waitcnt lgkmcnt(9)\n"
    "ds_read_b64_tr_b16 v[106:107], %7 offset:7744\n"
    "v_add_u32 %9, %13, %9\n"
    "v_mfma_f32_16x16x32_bf16 v[232:235], v[88:91], v[112:115], v[232:235]\n"
    "s_cmp_lt_u32 %10, 10\n"
    "ds_read_b64_tr_b16 v[108:109], %7 offset:96\n"
    "v_mfma_f32_16x16x32_bf16 v[236:239], v[92:95], v[112:115], v[236:239]\n"
    "s_waitcnt lgkmcnt(9)\n"
    "ds_read_b64_tr_b16 v[110:111], %7 offset:7776\n"
    "v_mfma_f32_16x16x32_bf16 v[240:243], v[88:91], v[116:119], v[240:243]\n"
    "ds_read_b64_tr_b16 v[112:113], %7 offset:128\n"
    "v_mfma_f32_16x16x32_bf16 v[244:247], v[92:95], v[116:119], v[244:247]\n"
    "s_waitcnt lgkmcnt(4)\n"
    "ds_read_b64_tr_b16 v[114:115], %7 offset:7808\n"
    "s_cbranch_scc1 1b\n"
    "s_waitcnt vmcnt(0) lgkmcnt(0)\n"
    "s_nop 7\n"
    "v_mov_b32 v124, 0x3f803f80\n"
    "v_mov_b32 v125, 0x3f803f80\n"
    "v_mov_b32 v126, 0x3f803f80\n"
    "v_mov_b32 v127, 0x3f803f80\n"
    "v_cvt_pk_bf16_f32 v80, v128, v129\n"
    "v_cvt_pk_bf16_f32 v81, v130, v131\n"
    "v_cvt_pk_bf16_f32 v82, v132, v133\n"
    "v_cvt_pk_bf16_f32 v83, v134, v135\n"
    "ds_write_b128 v72, v[80:83] offset:0\n"
    "v_cvt_pk_bf16_f32 v84, v136, v137\n"
    "v_cvt_pk_bf16_f32 v85, v138, v139\n"
    "v_cvt_pk_bf16_f32 v86, v140, v141\n"
    "v_cvt_pk_bf16_f32 v87, v142, v143\n"
    "ds_write_b128 v73, v[84:87] offset:0\n"
    "v_cvt_pk_bf16_f32 v88, v144, v145\n"
    "v_cvt_pk_bf16_f32 v89, v146, v147\n"
    "v_cvt_pk_bf16_f32 v90, v148, v149\n"
    "v_cvt_pk_bf16_f32 v91, v150, v151\n"
    "ds_write_b128 v74, v[88:91] offset:0\n"
    "v_cvt_pk_bf16_f32 v92, v152, v153\n"
    "v_cvt_pk_bf16_f32 v93, v154, v155\n"
    "v_cvt_pk_bf16_f32 v94, v156, v157\n"
    "v_cvt_pk_bf16_f32 v95, v158, v159\n"
    "ds_write_b128 v75, v[92:95] offset:0\n"
    "v_cvt_pk_bf16_f32 v96, v160, v161\n"
    "v_cvt_pk_bf16_f32 v97, v162, v163\n"
    "v_cvt_pk_bf16_f32 v98, v164, v165\n"
    "v_cvt_pk_bf16_f32 v99, v166, v167\n"
    "ds_write_b128 v76, v[96:99] offset:0\n"
    "s_mov_b32 exec_lo, 15\n"
    "s_mov_b32 exec_hi, 15\n"
    "ds_write_b128 v77, v[124:127] offset:0\n"
    "s_mov_b64 exec, -1\n"
    "s_waitcnt lgkmcnt(0)\n"
    "s_barrier\n"
    "v_cvt_pk_bf16_f32 v100, v168, v169\n"
    "v_cvt_pk_bf16_f32 v101, v170, v171\n"
    "v_cvt_pk_bf16_f32 v102, v172, v173\n"
    "v_cvt_pk_bf16_f32 v103, v174, v175\n"
    "ds_write_b128 v72, v[100:103] offset:20992\n"
    "v_cvt_pk_bf16_f32 v104, v176, v177\n"
    "v_cvt_pk_bf16_f32 v105, v178, v179\n"
    "v_cvt_pk_bf16_f32 v106, v180, v181\n"
    "v_cvt_pk_bf16_f32 v107, v182, v183\n"
    "ds_write_b128 v73, v[104:107] offset:20992\n"
    "v_cvt_pk_bf16_f32 v108, v184, v185\n"
    "v_cvt_pk_bf16_f32 v109, v186, v187\n"
    "v_cvt_pk_bf16_f32 v110, v188, v189\n"
    "v_cvt_pk_bf16_f32 v111, v190, v191\n"
    "ds_write_b128 v74, v[108:111] offset:20992\n"
    "v_cvt_pk_bf16_f32 v112, v192, v193\n"
    "v_cvt_pk_bf16_f32 v113, v194, v195\n"
    "v_cvt_pk_bf16_f32 v114, v196, v197\n"
    "v_cvt_pk_bf16_f32 v115, v198, v199\n"
    "ds_write_b128 v75, v[112:115] offset:20992\n"
    "v_cvt_pk_bf16_f32 v116, v200, v201\n"
    "v_cvt_pk_bf16_f32 v117, v202, v203\n"
    "v_cvt_pk_bf16_f32 v118, v204, v205\n"
    "v_cvt_pk_bf16_f32 v119, v206, v207\n"
    "ds_write_b128 v76, v[116:119] offset:20992\n"
    "s_mov_b32 exec_lo, 15\n"
    "s_mov_b32 exec_hi, 15\n"
    "ds_write_b128 v77, v[124:127] offset:20992\n"
    "s_mov_b64 exec, -1\n"
    "s_waitcnt lgkmcnt(0)\n"
    "s_barrier\n"
    "v_cvt_pk_bf16_f32 v80, v208, v209\n"
    "v_cvt_pk_bf16_f32 v81, v210, v211\n"
    "v_cvt_pk_bf16_f32 v82, v212, v213\n"
    "v_cvt_pk_bf16_f32 v83, v214, v215\n"
    "ds_write_b128 v72, v[80:83] offset:41984\n"
    "v_cvt_pk_bf16_f32 v84, v216, v217\n"
    "v_cvt_pk_bf16_f32 v85, v218, v219\n"
    "v_cvt_pk_bf16_f32 v86, v220, v221\n"
    "v_cvt_pk_bf16_f32 v87, v222, v223\n"
    "ds_write_b128 v73, v[84:87] offset:41984\n"
    "v_cvt_pk_bf16_f32 v88, v224, v225\n"
    "v_cvt_pk_bf16_f32 v89, v226, v227\n"
    "v_cvt_pk_bf16_f32 v90, v228, v229\n"
    "v_cvt_pk_bf16_f32 v91, v230, v231\n"
    "ds_write_b128 v74, v[88:91] offset:41984\n"
    "v_cvt_pk_bf16_f32 v92, v232, v233\n"
    "v_cvt_pk_bf16_f32 v93, v234, v235\n"
    "v_cvt_pk_bf16_f32 v94, v236, v237\n"
    "v_cvt_pk_bf16_f32 v95, v238, v239\n"
    "ds_write_b128 v75, v[92:95] offset:41984\n"
    "v_cvt_pk_bf16_f32 v96, v240, v241\n"
    "v_cvt_pk_bf16_f32 v97, v242, v243\n"
    "v_cvt_pk_bf16_f32 v98, v244, v245\n"
    "v_cvt_pk_bf16_f32 v99, v246, v247\n"
    "ds_write_b128 v76, v[96:99] offset:41984\n"
    "s_mov_b32 exec_lo, 15\n"
    "s_mov_b32 exec_hi, 15\n"
    "ds_write_b128 v77, v[124:127] offset:41984\n"
    "s_mov_b64 exec, -1\n"
    "s_waitcnt lgkmcnt(0)\n"
    "s_barrier\n");
    }
    LANES();
    if (!isA) {
        lptr src = lds + (8 + (w >> 1)) * K1_RS + (w & 1) * 3072 + l * 16;
        const u32x4 v0 = LLD16(src), v1 = LLD16(src + 1024), v2 = LLD16(src + 2048);
        st16(dlast, l * 16, v0, 0); st16(dlast, l * 16, v1, 1024);
        if (l < ((w & 1) ? (j >= 5 ? 8 : 0) : 64)) st16(dlast, l * 16, v2, 2048);
    } else {
    bf16x8 w0[5], w1[5], w2[5];
    {   lptr wq = lds + (lp == g ? K1_WPHI + g * 800 + lq * 40 : K1_ZERO); const s16x4 z4 = {0, 0, 0, 0};
#pragma unroll
        for (int t = 0; t < 5; ++t) { w0[t] = cat8(trrd(wq + 8 * t), trrd(wq + 160 + 8 * t)); w1[t] = cat8(trrd(wq + 320 + 8 * t), trrd(wq + 480 + 8 * t)); w2[t] = cat8(trrd(wq + 640 + 8 * t), z4); } }
    auto store_q = [&] { const char* b = ws + WS_Q + (size_t)(4 * j * 64 + ti * 4 + c) * 1280; const unsigned r = (w >> 2) * 16 + tau;
        st16(b, g * 64 * 1280 + r * 16, qb0); st16(b + 512, g * 64 * 1280 + r * 16, qb1); st8(b + 1024, g * 64 * 1280 + r * 8, u32x2{kq2[2], kq2[3]}); };
    if (w >= 4) { asm volatile("s_barrier" ::: "memory"); lptr src = lds + K1_XB + c * 5120 + l * 16; kb0 = LLD16(src); kb1 = LLD16(src + 1024); qb0 = LLD16(src + 2048); qb1 = LLD16(src + 3072); kq2 = LLD16(src + 4096); }
    else store_q();
    {   const bf16x8 b0 = __builtin_bit_cast(bf16x8, kb0), b1 = __builtin_bit_cast(bf16x8, kb1), b2 = __builtin_bit_cast(bf16x8, u32x4{kq2[0], kq2[1], 0u, 0u});
        f32x2 p[10];
#pragma unroll
        for (int t = 0; t < 5; ++t) { f32x4 cq = mf16(w2[t], b2, f32x4{0, 0, 0, 0}); cq = mf16(w0[t], b0, cq); cq = mf16(w1[t], b1, cq);
            p[2 * t] = f32x2{cq[0], cq[1]}; p[2 * t + 1] = f32x2{cq[2], cq[3]}; }
        float mx = __builtin_fmaxf(p[0][0], p[0][1]), mn = __builtin_fminf(p[0][0], p[0][1]);
#pragma unroll
        for (int i = 1; i < 10; ++i) { mx = max3f(mx, p[i][0], p[i][1]); mn = min3f(mn, p[i][0], p[i][1]); }
        const f32x2 cl = {LOG2E, LOG2E}, bx = {-mx * LOG2E, -mx * LOG2E}, bn = {mn * LOG2E, mn * LOG2E}; f32x2 e[10], n[10], se = {0.f, 0.f}, sn = {0.f, 0.f};
#pragma unroll
        for (int i = 0; i < 10; ++i) { const f32x2 a = __builtin_elementwise_fma(p[i], cl, bx), b = __builtin_elementwise_fma(p[i], -cl, bn);
            e[i] = f32x2{__builtin_amdgcn_exp2f(a[0]), __builtin_amdgcn_exp2f(a[1])}; n[i] = f32x2{__builtin_amdgcn_exp2f(b[0]), __builtin_amdgcn_exp2f(b[1])}; se += e[i]; sn += n[i]; }
        const float re = __builtin_amdgcn_rcpf(se[0] + se[1]), rn = __builtin_amdgcn_rcpf(sn[0] + sn[1]);
        unsigned q[20];
#pragma unroll
        for (int i = 0; i < 10; ++i) { const f32x2 a = e[i] * re, b = n[i] * rn; q[i] = pk2(a[0], a[1]); q[10 + i] = pk2(b[0], b[1]); }
        lptr dst = lds + K1_PH + (g * 128 + c * 32 + (w >> 2) * 16 + tau) * 80;
#pragma unroll
        for (int i = 0; i < 5; ++i) LST16(dst + 16 * i, u32x4{q[4 * i], q[4 * i + 1], q[4 * i + 2], q[4 * i + 3]});
    }
    if (w >= 4) store_q();
    asm volatile("s_waitcnt lgkmcnt(0)\n\ts_barrier" ::: "memory");
    {
        const int nt = w >> 2;
        lptr pa = lds + K1_PH + (c * 128 + 8 * g + lq) * 80 + 8 * lp;
        lptr pb = lds + K1_VL + (c * 128 + 8 * g + lq) * 48 + 8 * lp + 32 * nt;
        f32x4 c0 = {0, 0, 0, 0}, c1 = {0, 0, 0, 0}, c2 = {0, 0, 0, 0}; bf16x8 fb[4], fa[4][3];
#pragma unroll
        for (int ks = 0; ks < 4; ++ks) { fb[ks] = cat8(trrd(pb + ks * 32 * 48), trrd(pb + ks * 32 * 48 + 4 * 48));
#pragma unroll
            for (int m = 0; m < 3; ++m) fa[ks][m] = cat8(trrd(pa + ks * 2560 + 32 * m), trrd(pa + ks * 2560 + 320 + 32 * m)); }
        __builtin_amdgcn_sched_barrier(0);
#pragma unroll
        for (int ks = 0; ks < 4; ++ks) { c0 = mf16(fa[ks][0], fb[ks], c0); c1 = mf16(fa[ks][1], fb[ks], c1); c2 = mf16(fa[ks][2], fb[ks], c2); }
        char* dst = ws + WS_KVP + (size_t)((4 * j + c) * 16 + ti) * KVPS + nt * 2560; const unsigned o = (nt ? g * 5 + tau : l) * 16, sz = nt ? 320 : 1024;
        if (16 * nt + tau < 21) { st16(dst, o, __builtin_bit_cast(u32x4, c0)); st16(dst + sz, o, __builtin_bit_cast(u32x4, c1)); if (g < 2) st16(dst + 2 * sz, o, __builtin_bit_cast(u32x4, c2)); }
    }
    }
    tag += 0x9e3779b97f4a7c15ull;
    if (!isA) asm volatile("s_waitcnt vmcnt(0)" ::: "memory");
    asm volatile("s_waitcnt lgkmcnt(0)\n\ts_barrier" ::: "memory");
    if (w == 0 && !isA) { const int qd = 3 * j + l; if (l < 3) post(ctl, CTL_FB + ((qd & 7) * 64 + (qd >> 3) * 16 + ti) * FS, tag); }
    LANES();
    int r = l & 31; const int hh = l >> 5;
    const int h = (blockIdx.x & 7) * 4 + ((blockIdx.x >> 3) & 3), tt = (blockIdx.x >> 5) * 8 + 2 * c;
    const char* qg = ws + WS_Q + (w < 4 ? 2621440 : 0) + (size_t)(h * 64 + tt) * 1280;
    const char* kvh = ws + WS_KV + (size_t)h * 16 * KVB;
    const u32x2 z2 = {0, 0};
    const f32x16 zero16 = {0, 0, 0, 0, 0, 0, 0, 0, 0, 0, 0, 0, 0, 0, 0, 0};
    const char* kv0g = kvh + w * 4096; unsigned l16 = l * 16, r8 = r * 8;
    asm volatile("" : "+s"(qg), "+s"(kv0g)); asm volatile("" : "+v"(l16), "+v"(r8));
    if (w == 1) await(ctl, CTL_FB + ((h >> 2) * 64 + ((l & 8) ? 2 * (blockIdx.x >> 5) + (l & 1) : 16 + (l & 3) + 4 * (l & 4))) * FS, tag);
    asm volatile("s_barrier" ::: "memory");
    if (w >= 4) {
        u32x4 qa1, qb1; u32x2 qa2, qb2;
        __builtin_amdgcn_s_setprio(2);
        const char* src = kv0g; unsigned dst = ldsb + w * 4096;
        auto stream = [&](int n) {
#pragma unroll 1
            for (int i = 0; i < n; ++i) { chunk4k(src, l16, dst); src += 16384; dst += 16384; } };
        stream(1);
        asm volatile("s_barrier" ::: "memory");
        asm volatile("s_waitcnt vmcnt(0)\n\ts_barrier" ::: "memory");
        if (isA && c == 0 && l == 0) post(ctl, CTL_FA + (j * 16 + ti) * FS, tag);
        await(ctl, CTL_FB + ((h >> 2) * 64 + 16 + (l & 31)) * FS, tag);
        stream(2);
        asm volatile("s_waitcnt vmcnt(0)\n\ts_barrier" ::: "memory");
        stream(2);
        chunk4k(kvh + 14 * KVB + c * 4096, l * 16, ldsb + c * 4096);
        chunk1k(kvh + 14 * KVB + 16384 + c * 1024, l * 16, ldsb + 16384 + c * 1024);
        if (c == 0 && l < 16) chunk1k(kvh + 14 * KVB + 20480, l * 16, ldsb + 20480);
        asm volatile("s_waitcnt vmcnt(5)\n\ts_barrier" ::: "memory");
        stream(3);
        await(ctl, CTL_FA + ((h >> 2) * 16 + (l & 15)) * FS, tag);
        const int ec = c * 53 + l < 210 ? c * 53 + l : 209;
        const char* pk = ws + WS_KVP + (size_t)h * 16 * KVPS;
        f32x4 part[16];
        glds16m(wphi + h * 800, (l < 50 ? l : 49) * 16, ldsb + K2_WP + c * 1024);
#pragma unroll
        for (int i = 0; i < 16; ++i) asm volatile("global_load_dwordx4 %0, %1, %2 sc1" : "=v"(part[i]) : "v"(ec * 16), "s"(pk + i * KVPS));
        asm volatile("global_load_dwordx4 %0, %1, %2 sc1" : "=v"(qa1) : "v"(l * 16), "s"(qg));
        asm volatile("global_load_dwordx2 %0, %1, %2 offset:1024 sc1" : "=v"(qa2) : "v"(r * 8), "s"(qg));
        asm volatile("global_load_dwordx4 %0, %1, %2 offset:1280 sc1" : "=v"(qb1) : "v"(l * 16), "s"(qg));
        asm volatile("global_load_dwordx2 %0, %1, %2 offset:2304 sc1" : "=v"(qb2) : "v"(r * 8), "s"(qg));
        asm volatile("s_waitcnt vmcnt(0)" : "+v"(part[0]), "+v"(part[1]), "+v"(part[2]), "+v"(part[3]), "+v"(part[4]), "+v"(part[5]), "+v"(part[6]), "+v"(part[7]),
                     "+v"(part[8]), "+v"(part[9]), "+v"(part[10]), "+v"(part[11]), "+v"(part[12]), "+v"(part[13]), "+v"(part[14]), "+v"(part[15]),
                     "+v"(qa1), "+v"(qa2), "+v"(qb1), "+v"(qb2) :: "memory");
        qa2 = hh ? z2 : qa2; qb2 = hh ? z2 : qb2;
        const f32x4 sum = (((part[0] + part[1]) + (part[2] + part[3])) + ((part[4] + part[5]) + (part[6] + part[7]))) + (((part[8] + part[9]) + (part[10] + part[11])) + ((part[12] + part[13]) + (part[14] + part[15])));
        {   const int e1 = ec - 160, m = ec < 160 ? ec >> 6 : (e1 * 52) >> 10, e2 = e1 - 20 * m, g5 = (e2 * 52) >> 8;
            const int d = ec < 160 ? ec & 15 : 16 + e2 - 5 * g5, f0 = 16 * m + 4 * (ec < 160 ? (ec >> 4) & 3 : g5);
            if (l < 53) LST8(lds + K2_TMP + d * 80 + f0 * 2, u32x2{pk2(sum[0], sum[1]), pk2(sum[2], sum[3])}); }
        lptr wp = lds + K2_WP + c * 1024 + lq * 40; const unsigned sgn = (l & 4) ? 0x80008000u : 0u; const int f00 = 16 * (g & 1) + 8 * (lp >> 1);
        u32x4 w00 = __builtin_bit_cast(u32x4, cat8(trrd(wp + hh * 320 + f00), trrd(wp + hh * 320 + 160 + f00))) ^ sgn, w10 = __builtin_bit_cast(u32x4, cat8(trrd(wp + hh * 320 + 32), trrd(wp + hh * 320 + 192))) ^ sgn;
        u32x2 w01 = __builtin_bit_cast(u32x2, trrd(wp + 640 + f00)) ^ sgn, w11 = __builtin_bit_cast(u32x2, trrd(wp + 672)) ^ sgn;
        asm volatile("s_waitcnt lgkmcnt(0)\n\ts_barrier" : "+v"(w00), "+v"(w01), "+v"(w10), "+v"(w11), "+v"(qa2), "+v"(qb2) :: "memory");
        lptr kp = lds + K2_TMP + r * 80 + hh * 40;
        bf16x8 kv0 = cat8(ld4(kp), ld4(kp + 8)), kv1 = cat8(ld4(kp + 16), ld4(kp + 24)); s16x4 kv2 = ld4(kp + 32);
        asm volatile("" : "+v"(kv0), "+v"(kv1), "+v"(kv2));
        const int un = c * 256 + blockIdx.x;
        if (un < 320) { const int ub = un >= 160, uj = (un - 160 * ub) / 20, us = un - 160 * ub - 20 * uj, rr = (l * 26) >> 8;
            const char* wsrc = (ub ? woB : woA) + uj * 160;
            if (l < 40) {
#pragma unroll 1
                for (int i = 0; i < 8; ++i) { const int gq = i & 3, b = i >> 2, row = us < 16 ? (2 * us + (gq >> 1)) * 20 + 8 * (gq & 1) + 4 * b : (8 * (us - 16) + 2 * gq + b) * 20 + 16;
                    glds16m(wsrc + row * 1280, rr * 1280 + (l - 10 * rr) * 16, ldsb + K2_WR + c * 5120 + i * 640); } }
            asm volatile("s_waitcnt vmcnt(0)" ::: "memory");
            lptr wb = lds + K2_WR + c * 5120 + (4 * g + lq) * 160 + 40 * lp;
#pragma unroll
            for (int t = 0; t < 5; ++t) st16(ws + WS_WOF + un * 5120 + t * 1024, l * 16, __builtin_bit_cast(u32x4, cat8(trrd(wb + 8 * t), trrd(wb + 2560 + 8 * t))));
            asm volatile("s_waitcnt vmcnt(0)" ::: "memory");
            if (l == 0) post(ctl, CTL_FW + un * FS, tag);
        }
        await(ctl, CTL_FW + (((xcd >> 2) * 8 + (slot & 7)) * 20 + (l < 20 ? l : 0)) * FS, tag);
        asm volatile("s_barrier" ::: "memory");
        src = ws + WS_WOF + (size_t)((xcd >> 2) * 8 + (slot & 7)) * 102400;
#pragma unroll 1
        for (int i = c; i < 25; i += 4) chunk4k(src + i * 4096, l * 16, ldsb + K3_WO + i * 4096);
        __builtin_amdgcn_s_setprio(0);
        unsigned long long cl;
        asm volatile(
    "s_mov_b64 %0, 0x3fb8aa3b\n"
    "v_mfma_f32_32x32x8_bf16 v[144:159], %4, %9, 0\n"
    "v_mfma_f32_32x32x8_bf16 v[176:191], %4, %11, 0\n"
    "v_mfma_f32_32x32x8_bf16 v[128:143], %2, %9, 0\n"
    "v_mfma_f32_32x32x8_bf16 v[160:175], %2, %11, 0\n"
    "s_nop 6\n"
    "v_mfma_f32_32x32x16_bf16 v[144:159], %3, %8, v[144:159]\n"
    "v_mfma_f32_32x32x16_bf16 v[176:191], %3, %10, v[176:191]\n"
    "v_mfma_f32_32x32x16_bf16 v[128:143], %1, %8, v[128:143]\n"
    "v_mfma_f32_32x32x16_bf16 v[160:175], %1, %10, v[160:175]\n"
    "s_nop 9\n"
    "v_max3_f32 v224, v144, v145, v146\n"
    "v_max3_f32 v240, v176, v177, v178\n"
    "v_max3_f32 v225, v147, v128, v129\n"
    "v_max3_f32 v241, v179, v160, v161\n"
    "v_max3_f32 v224, v224, v130, v131\n"
    "v_max3_f32 v240, v240, v162, v163\n"
    "v_max3_f32 v225, v225, v132, v133\n"
    "v_max3_f32 v241, v241, v164, v165\n"
    "v_max3_f32 v224, v224, v134, v135\n"
    "v_max3_f32 v240, v240, v166, v167\n"
    "v_max3_f32 v225, v225, v136, v137\n"
    "v_max3_f32 v241, v241, v168, v169\n"
    "v_max3_f32 v224, v224, v138, v139\n"
    "v_max3_f32 v240, v240, v170, v171\n"
    "v_max3_f32 v225, v225, v140, v141\n"
    "v_max3_f32 v241, v241, v172, v173\n"
    "v_max3_f32 v224, v224, v142, v143\n"
    "v_max3_f32 v240, v240, v174, v175\n"
    "v_max_f32 v224, v224, v225\n"
    "v_max_f32 v240, v240, v241\n"
    "v_mul_f32 v224, 0xbfb8aa3b, v224\n"
    "v_mul_f32 v240, 0xbfb8aa3b, v240\n"
    "v_pk_fma_f32 v[128:129], v[128:129], %0, v[224:225] op_sel_hi:[1,0,0]\n"
    "v_pk_fma_f32 v[160:161], v[160:161], %0, v[240:241] op_sel_hi:[1,0,0]\n"
    "v_pk_fma_f32 v[130:131], v[130:131], %0, v[224:225] op_sel_hi:[1,0,0]\n"
    "v_pk_fma_f32 v[162:163], v[162:163], %0, v[240:241] op_sel_hi:[1,0,0]\n"
    "v_pk_fma_f32 v[132:133], v[132:133], %0, v[224:225] op_sel_hi:[1,0,0]\n"
    "v_pk_fma_f32 v[164:165], v[164:165], %0, v[240:241] op_sel_hi:[1,0,0]\n"
    "v_pk_fma_f32 v[134:135], v[134:135], %0, v[224:225] op_sel_hi:[1,0,0]\n"
    "v_pk_fma_f32 v[166:167], v[166:167], %0, v[240:241] op_sel_hi:[1,0,0]\n"
    "v_pk_fma_f32 v[136:137], v[136:137], %0, v[224:225] op_sel_hi:[1,0,0]\n"
    "v_pk_fma_f32 v[168:169], v[168:169], %0, v[240:241] op_sel_hi:[1,0,0]\n"
    "v_pk_fma_f32 v[138:139], v[138:139], %0, v[224:225] op_sel_hi:[1,0,0]\n"
    "v_pk_fma_f32 v[170:171], v[170:171], %0, v[240:241] op_sel_hi:[1,0,0]\n"
    "v_pk_fma_f32 v[140:141], v[140:141], %0, v[224:225] op_sel_hi:[1,0,0]\n"
    "v_pk_fma_f32 v[172:173], v[172:173], %0, v[240:241] op_sel_hi:[1,0,0]\n"
    "v_pk_fma_f32 v[142:143], v[142:143], %0, v[224:225] op_sel_hi:[1,0,0]\n"
    "v_pk_fma_f32 v[174:175], v[174:175], %0, v[240:241] op_sel_hi:[1,0,0]\n"
    "v_pk_fma_f32 v[144:145], v[144:145], %0, v[224:225] op_sel_hi:[1,0,0]\n"
    "v_pk_fma_f32 v[176:177], v[176:177], %0, v[240:241] op_sel_hi:[1,0,0]\n"
    "v_pk_fma_f32 v[146:147], v[146:147], %0, v[224:225] op_sel_hi:[1,0,0]\n"
    "v_pk_fma_f32 v[178:179], v[178:179], %0, v[240:241] op_sel_hi:[1,0,0]\n"
    "v_exp_f32 v128, v128\n"
    "v_exp_f32 v160, v160\n"
    "v_exp_f32 v129, v129\n"
    "v_exp_f32 v161, v161\n"
    "v_exp_f32 v130, v130\n"
    "v_exp_f32 v162, v162\n"
    "v_exp_f32 v131, v131\n"
    "v_exp_f32 v163, v163\n"
    "v_exp_f32 v132, v132\n"
    "v_exp_f32 v164, v164\n"
    "v_exp_f32 v133, v133\n"
    "v_exp_f32 v165, v165\n"
    "v_exp_f32 v134, v134\n"
    "v_exp_f32 v166, v166\n"
    "v_exp_f32 v135, v135\n"
    "v_exp_f32 v167, v167\n"
    "v_exp_f32 v136, v136\n"
    "v_exp_f32 v168, v168\n"
    "v_exp_f32 v137, v137\n"
    "v_exp_f32 v169, v169\n"
    "v_exp_f32 v138, v138\n"
    "v_exp_f32 v170, v170\n"
    "v_exp_f32 v139, v139\n"
    "v_exp_f32 v171, v171\n"
    "v_exp_f32 v140, v140\n"
    "v_exp_f32 v172, v172\n"
    "v_exp_f32 v141, v141\n"
    "v_exp_f32 v173, v173\n"
    "v_exp_f32 v142, v142\n"
    "v_exp_f32 v174, v174\n"
    "v_exp_f32 v143, v143\n"
    "v_exp_f32 v175, v175\n"
    "v_exp_f32 v144, v144\n"
    "v_exp_f32 v176, v176\n"
    "v_exp_f32 v145, v145\n"
    "v_exp_f32 v177, v177\n"
    "v_exp_f32 v146, v146\n"
    "v_exp_f32 v178, v178\n"
    "v_exp_f32 v147, v147\n"
    "v_exp_f32 v179, v179\n"
    "v_pk_add_f32 v[224:225], v[128:129], v[130:131]\n"
    "v_pk_add_f32 v[240:241], v[160:161], v[162:163]\n"
    "v_pk_add_f32 v[226:227], v[132:133], v[134:135]\n"
    "v_pk_add_f32 v[242:243], v[164:165], v[166:167]\n"
    "v_pk_add_f32 v[224:225], v[224:225], v[136:137]\n"
    "v_pk_add_f32 v[240:241], v[240:241], v[168:169]\n"
    "v_pk_add_f32 v[226:227], v[226:227], v[138:139]\n"
    "v_pk_add_f32 v[242:243], v[242:243], v[170:171]\n"
    "v_pk_add_f32 v[224:225], v[224:225], v[140:141]\n"
    "v_pk_add_f32 v[240:241], v[240:241], v[172:173]\n"
    "v_pk_add_f32 v[226:227], v[226:227], v[142:143]\n"
    "v_pk_add_f32 v[242:243], v[242:243], v[174:175]\n"
    "v_pk_add_f32 v[224:225], v[224:225], v[144:145]\n"
    "v_pk_add_f32 v[240:241], v[240:241], v[176:177]\n"
    "v_pk_add_f32 v[226:227], v[226:227], v[146:147]\n"
    "v_pk_add_f32 v[242:243], v[242:243], v[178:179]\n"
    "v_pk_add_f32 v[224:225], v[224:225], v[226:227]\n"
    "v_pk_add_f32 v[240:241], v[240:241], v[242:243]\n"
    "v_add_f32 v224, v224, v225\n"
    "v_add_f32 v240, v240, v241\n"
    "v_rcp_f32 v224, v224\n"
    "v_rcp_f32 v240, v240\n"
    "v_pk_mul_f32 v[128:129], v[128:129], v[224:225] op_sel_hi:[1,0]\n"
    "v_pk_mul_f32 v[160:161], v[160:161], v[240:241] op_sel_hi:[1,0]\n"
    "v_pk_mul_f32 v[130:131], v[130:131], v[224:225] op_sel_hi:[1,0]\n"
    "v_pk_mul_f32 v[162:163], v[162:163], v[240:241] op_sel_hi:[1,0]\n"
    "v_pk_mul_f32 v[132:133], v[132:133], v[224:225] op_sel_hi:[1,0]\n"
    "v_pk_mul_f32 v[164:165], v[164:165], v[240:241] op_sel_hi:[1,0]\n"
    "v_pk_mul_f32 v[134:135], v[134:135], v[224:225] op_sel_hi:[1,0]\n"
    "v_pk_mul_f32 v[166:167], v[166:167], v[240:241] op_sel_hi:[1,0]\n"
    "v_pk_mul_f32 v[136:137], v[136:137], v[224:225] op_sel_hi:[1,0]\n"
    "v_pk_mul_f32 v[168:169], v[168:169], v[240:241] op_sel_hi:[1,0]\n"
    "v_pk_mul_f32 v[138:139], v[138:139], v[224:225] op_sel_hi:[1,0]\n"
    "v_pk_mul_f32 v[170:171], v[170:171], v[240:241] op_sel_hi:[1,0]\n"
    "v_pk_mul_f32 v[140:141], v[140:141], v[224:225] op_sel_hi:[1,0]\n"
    "v_pk_mul_f32 v[172:173], v[172:173], v[240:241] op_sel_hi:[1,0]\n"
    "v_pk_mul_f32 v[142:143], v[142:143], v[224:225] op_sel_hi:[1,0]\n"
    "v_pk_mul_f32 v[174:175], v[174:175], v[240:241] op_sel_hi:[1,0]\n"
    "v_pk_mul_f32 v[144:145], v[144:145], v[224:225] op_sel_hi:[1,0]\n"
    "v_pk_mul_f32 v[176:177], v[176:177], v[240:241] op_sel_hi:[1,0]\n"
    "v_pk_mul_f32 v[146:147], v[146:147], v[224:225] op_sel_hi:[1,0]\n"
    "v_pk_mul_f32 v[178:179], v[178:179], v[240:241] op_sel_hi:[1,0]\n"
    "v_cvt_pk_bf16_f32 v236, v144, v145\n"
    "v_cvt_pk_bf16_f32 v252, v176, v177\n"
    "v_cvt_pk_bf16_f32 v237, v146, v147\n"
    "v_cvt_pk_bf16_f32 v253, v178, v179\n"
    "v_cvt_pk_bf16_f32 v228, v128, v129\n"
    "v_cvt_pk_bf16_f32 v244, v160, v161\n"
    "v_cvt_pk_bf16_f32 v229, v130, v131\n"
    "v_cvt_pk_bf16_f32 v245, v162, v163\n"
    "v_cvt_pk_bf16_f32 v230, v132, v133\n"
    "v_cvt_pk_bf16_f32 v246, v164, v165\n"
    "v_cvt_pk_bf16_f32 v231, v134, v135\n"
    "v_cvt_pk_bf16_f32 v247, v166, v167\n"
    "v_cvt_pk_bf16_f32 v232, v136, v137\n"
    "v_cvt_pk_bf16_f32 v248, v168, v169\n"
    "v_cvt_pk_bf16_f32 v233, v138, v139\n"
    "v_cvt_pk_bf16_f32 v249, v170, v171\n"
    "v_cvt_pk_bf16_f32 v234, v140, v141\n"
    "v_cvt_pk_bf16_f32 v250, v172, v173\n"
    "v_cvt_pk_bf16_f32 v235, v142, v143\n"
    "v_cvt_pk_bf16_f32 v251, v174, v175\n"
    "s_nop 0\n"
    "v_mfma_f32_32x32x8_bf16 v[192:207], %7, v[236:237], 0\n"
    "v_mfma_f32_32x32x8_bf16 v[208:223], %7, v[252:253], 0\n"
    "s_nop 8\n"
    "v_mfma_f32_32x32x16_bf16 v[192:207], %5, v[228:231], v[192:207]\n"
    "v_mfma_f32_32x32x16_bf16 v[208:223], %5, v[244:247], v[208:223]\n"
    "v_mfma_f32_32x32x16_bf16 v[192:207], %6, v[232:235], v[192:207]\n"
    "v_mfma_f32_32x32x16_bf16 v[208:223], %6, v[248:251], v[208:223]\n"
    "s_nop 10\n"
    "v_mov_b32 v224, v200\n"
    "v_mov_b32 v225, v200\n"
    "v_mov_b32 v226, v216\n"
    "v_mov_b32 v227, v216\n"
    "s_nop 0\n"
    "v_permlane32_swap_b32 v224, v225\n"
    "v_permlane32_swap_b32 v226, v227\n"
    "v_add_f32 v225, 0x358637bd, v225\n"
    "v_add_f32 v227, 0x358637bd, v227\n"
    "v_rcp_f32 v224, v225\n"
    "v_rcp_f32 v226, v227\n"
    "s_nop 0\n"
    "v_pk_mul_f32 v[192:193], v[192:193], v[224:225] op_sel_hi:[1,0]\n"
    "v_pk_mul_f32 v[208:209], v[208:209], v[226:227] op_sel_hi:[1,0]\n"
    "v_pk_mul_f32 v[194:195], v[194:195], v[224:225] op_sel_hi:[1,0]\n"
    "v_pk_mul_f32 v[210:211], v[210:211], v[226:227] op_sel_hi:[1,0]\n"
    "v_pk_mul_f32 v[196:197], v[196:197], v[224:225] op_sel_hi:[1,0]\n"
    "v_pk_mul_f32 v[212:213], v[212:213], v[226:227] op_sel_hi:[1,0]\n"
    "v_pk_mul_f32 v[198:199], v[198:199], v[224:225] op_sel_hi:[1,0]\n"
    "v_pk_mul_f32 v[214:215], v[214:215], v[226:227] op_sel_hi:[1,0]\n"
    "v_pk_mul_f32 v[200:201], v[200:201], v[224:225] op_sel_hi:[1,0]\n"
    "v_pk_mul_f32 v[216:217], v[216:217], v[226:227] op_sel_hi:[1,0]\n"
    "v_pk_mul_f32 v[202:203], v[202:203], v[224:225] op_sel_hi:[1,0]\n"
    "v_pk_mul_f32 v[218:219], v[218:219], v[226:227] op_sel_hi:[1,0]\n"
    "v_cvt_pk_bf16_f32 v240, v192, v193\n"
    "v_cvt_pk_bf16_f32 v248, v208, v209\n"
    "v_cvt_pk_bf16_f32 v242, v196, v197\n"
    "v_cvt_pk_bf16_f32 v250, v212, v213\n"
    "v_cvt_pk_bf16_f32 v241, v194, v195\n"
    "v_cvt_pk_bf16_f32 v249, v210, v211\n"
    "v_cvt_pk_bf16_f32 v243, v198, v199\n"
    "v_cvt_pk_bf16_f32 v251, v214, v215\n"
    "v_cvt_pk_bf16_f32 v244, v200, v201\n"
    "v_cvt_pk_bf16_f32 v252, v216, v217\n"
    "v_cvt_pk_bf16_f32 v245, v202, v203\n"
    "v_cvt_pk_bf16_f32 v253, v218, v219\n"
    "v_permlane32_swap_b32 v240, v242\n"
    "v_permlane32_swap_b32 v248, v250\n"
    "v_permlane32_swap_b32 v241, v243\n"
    "v_permlane32_swap_b32 v249, v251\n"
    "v_add_u32 v246, 40960, %13\n"
    "v_add_u32 v247, 40960, %14\n"
    "global_store_dwordx4 %13, v[240:243], %12 sc1\n"
    "global_store_dwordx4 v246, v[248:251], %12 sc1\n"
    "s_mov_b32 exec_hi, 0\n"
    "global_store_dwordx2 %14, v[244:245], %12 offset:1024 sc1\n"
    "global_store_dwordx2 v247, v[252:253], %12 offset:1024 sc1\n"
    "s_mov_b32 exec_hi, -1\n"
            : "=&s"(cl) : "v"(w00), "v"(w01), "v"(w10), "v"(w11), "v"(kv0), "v"(kv1), "v"(kv2), "v"(qa1), "v"(qa2), "v"(qb1), "v"(qb2),
              "s"(ws + WS_O + (size_t)tt * 40960 + h * 1280), "v"(l * 16), "v"(r * 8) : "memory", "v128", "v129", "v130", "v131", "v132", "v133", "v134", "v135", "v136", "v137", "v138", "v139", "v140", "v141", "v142", "v143", "v144", "v145", "v146", "v147", "v148", "v149", "v150", "v151", "v152", "v153", "v154", "v155", "v156", "v157", "v158", "v159", "v160", "v161", "v162", "v163", "v164", "v165", "v166", "v167", "v168", "v169", "v170", "v171", "v172", "v173", "v174", "v175", "v176", "v177", "v178", "v179", "v180", "v181", "v182", "v183", "v184", "v185", "v186", "v187", "v188", "v189", "v190", "v191", "v192", "v193", "v194", "v195", "v196", "v197", "v198", "v199", "v200", "v201", "v202", "v203", "v204", "v205", "v206", "v207", "v208", "v209", "v210", "v211", "v212", "v213", "v214", "v215", "v216", "v217", "v218", "v219", "v220", "v221", "v222", "v223", "v224", "v225", "v226", "v227", "v228", "v229", "v230", "v231", "v232", "v233", "v234", "v235", "v236", "v237", "v238", "v239", "v240", "v241", "v242", "v243", "v244", "v245", "v246", "v247", "v248", "v249", "v250", "v251", "v252", "v253", "v254", "v255");
    } else {
    bf16x8 a1, b1; s16x4 a2l, a2h, b2l, b2h;
    asm volatile("global_load_dwordx4 %0, %7, %6 sc1\n\tglobal_load_dwordx4 %1, %7, %6 offset:1280 sc1\n\t"
                 "v_mov_b64 %2, 0\n\tv_mov_b64 %3, 0\n\tv_mov_b64 %4, 0\n\tv_mov_b64 %5, 0\n\ts_mov_b32 exec_hi, 0\n\t"
                 "global_load_dwordx2 %2, %8, %6 offset:1024 sc1\n\tglobal_load_dwordx2 %4, %8, %6 offset:2304 sc1\n\ts_not_b64 exec, exec\n\t"
                 "global_load_dwordx2 %3, %8, %6 offset:1024 sc1\n\tglobal_load_dwordx2 %5, %8, %6 offset:2304 sc1\n\ts_mov_b64 exec, -1"
                 : "=&{v[80:83]}"(a1), "=&{v[84:87]}"(b1), "=&{v[88:89]}"(a2l), "=&{v[90:91]}"(a2h), "=&{v[92:93]}"(b2l), "=&{v[94:95]}"(b2h) : "s"(qg), "v"(l16), "v"(r8) : "memory", "scc");
    chunk4k(kv0g, l16, ldsb + w * 4096);
    f32x16 oa, ob; float la, lb; unsigned long long bad;
    {   unsigned ak = ldsb + l * 16, av = ldsb + (r < 20 ? hh * 320 + r * 16 : (r == 20 ? 640 : 0x30000)), it, inc;
        asm volatile(
    "s_mov_b32 %4, 0\n"
    "v_mov_b64 v[96:97], 0\n"
    "v_mov_b64 v[98:99], 0\n"
    "v_mov_b64 v[100:101], 0\n"
    "v_mov_b64 v[102:103], 0\n"
    "v_mov_b64 v[104:105], 0\n"
    "v_mov_b64 v[106:107], 0\n"
    "v_mov_b64 v[108:109], 0\n"
    "v_mov_b64 v[110:111], 0\n"
    "v_mov_b64 v[112:113], 0\n"
    "v_mov_b64 v[114:115], 0\n"
    "v_mov_b64 v[116:117], 0\n"
    "v_mov_b64 v[118:119], 0\n"
    "v_mov_b64 v[120:121], 0\n"
    "v_mov_b64 v[122:123], 0\n"
    "v_mov_b64 v[124:125], 0\n"
    "v_mov_b64 v[126:127], 0\n"
    "v_mov_b64 v[184:185], 0\n"
    "v_mov_b64 v[186:187], 0\n"
    "v_mov_b64 v[188:189], 0\n"
    "v_mov_b64 v[190:191], 0\n"
    "v_mov_b64 v[216:217], 0\n"
    "v_mov_b64 v[218:219], 0\n"
    "s_waitcnt vmcnt(4)\n"
    "s_cmp_gt_u32 %10, 1\n"
    "s_cbranch_scc1 3f\n"
    "s_waitcnt vmcnt(0)\n"
    "3:\n"
    "s_barrier\n"
    "ds_read_b128 v[200:203], %2 offset:4096\n"
    "ds_read_b128 v[192:195], %2 offset:0\n"
    "ds_read_b128 v[196:199], %2 offset:1024\n"
    "ds_read_b128 v[204:207], %3 offset:5120\n"
    "ds_read_b128 v[208:211], %3 offset:5776\n"
    "ds_read_b128 v[212:215], %3 offset:6432\n"
    "s_waitcnt lgkmcnt(4)\n"
    "v_mfma_f32_32x32x8_bf16 v[128:143], v[200:201], v[88:89], 0\n"
    "v_mfma_f32_32x32x8_bf16 v[160:175], v[200:201], v[92:93], 0\n"
    "v_mfma_f32_32x32x8_bf16 v[144:159], v[200:201], v[90:91], 0\n"
    "s_nop 7\n"
    "v_mfma_f32_32x32x16_bf16 v[128:143], v[192:195], v[80:83], v[128:143]\n"
    "v_mfma_f32_32x32x16_bf16 v[160:175], v[192:195], v[84:87], v[160:175]\n"
    "s_waitcnt lgkmcnt(3)\n"
    "v_mfma_f32_32x32x16_bf16 v[144:159], v[196:199], v[80:83], v[144:159]\n"
    "s_nop 8\n"
    "v_max3_f32 v252, v128, v129, v130\n"
    "v_max3_f32 v253, v131, v132, v133\n"
    "v_max3_f32 v252, v252, v134, v135\n"
    "v_max3_f32 v253, v253, v136, v137\n"
    "v_max3_f32 v252, v252, v138, v139\n"
    "v_max3_f32 v253, v253, v140, v141\n"
    "v_max3_f32 v252, v252, v142, v143\n"
    "v_max_f32 v252, v252, v253\n"
    "v_mov_b32 v253, v252\n"
    "v_max3_f32 v254, v160, v161, v162\n"
    "v_max3_f32 v255, v163, v164, v165\n"
    "v_max3_f32 v254, v254, v166, v167\n"
    "v_max3_f32 v255, v255, v168, v169\n"
    "v_max3_f32 v254, v254, v170, v171\n"
    "v_max3_f32 v255, v255, v172, v173\n"
    "v_max3_f32 v254, v254, v174, v175\n"
    "v_max_f32 v254, v254, v255\n"
    "v_mov_b32 v255, v254\n"
    "s_nop 0\n"
    "v_permlane32_swap_b32 v252, v253\n"
    "v_permlane32_swap_b32 v254, v255\n"
    "v_min_f32 v220, -v252, -v253\n"
    "v_min_f32 v236, -v254, -v255\n"
    "v_mov_b32 v221, v220\n"
    "v_mov_b32 v237, v236\n"
    "v_pk_add_f32 v[128:129], v[128:129], v[220:221]\n"
    "v_pk_add_f32 v[130:131], v[130:131], v[220:221]\n"
    "v_pk_add_f32 v[132:133], v[132:133], v[220:221]\n"
    "v_pk_add_f32 v[134:135], v[134:135], v[220:221]\n"
    "v_pk_add_f32 v[136:137], v[136:137], v[220:221]\n"
    "v_pk_add_f32 v[138:139], v[138:139], v[220:221]\n"
    "v_pk_add_f32 v[140:141], v[140:141], v[220:221]\n"
    "v_pk_add_f32 v[142:143], v[142:143], v[220:221]\n"
    "v_pk_add_f32 v[144:145], v[144:145], v[220:221]\n"
    "v_pk_add_f32 v[146:147], v[146:147], v[220:221]\n"
    "v_pk_add_f32 v[148:149], v[148:149], v[220:221]\n"
    "v_pk_add_f32 v[150:151], v[150:151], v[220:221]\n"
    "v_pk_add_f32 v[152:153], v[152:153], v[220:221]\n"
    "v_pk_add_f32 v[154:155], v[154:155], v[220:221]\n"
    "v_pk_add_f32 v[156:157], v[156:157], v[220:221]\n"
    "v_pk_add_f32 v[158:159], v[158:159], v[220:221]\n"
    "v_mov_b64 v[238:239], v[236:237]\n"
    "v_mov_b64 v[222:223], v[220:221]\n"
    "v_mov_b64 v[240:241], v[236:237]\n"
    "v_mov_b64 v[224:225], v[220:221]\n"
    "v_mov_b64 v[242:243], v[236:237]\n"
    "v_mov_b64 v[226:227], v[220:221]\n"
    "v_mov_b64 v[244:245], v[236:237]\n"
    "v_mov_b64 v[228:229], v[220:221]\n"
    "v_mov_b64 v[246:247], v[236:237]\n"
    "v_mov_b64 v[230:231], v[220:221]\n"
    "v_mov_b64 v[248:249], v[236:237]\n"
    "v_mov_b64 v[232:233], v[220:221]\n"
    "v_mov_b64 v[250:251], v[236:237]\n"
    "v_mov_b64 v[234:235], v[220:221]\n"
    "v_exp_f32 v128, v128\n"
    "v_exp_f32 v129, v129\n"
    "v_smfmac_f32_32x32x32_bf16 v[112:127], v[216:219], v[184:191], %9\n"
    "s_waitcnt vmcnt(0)\n"
    "1:\n"
    "s_barrier\n"
    "2:\n"
    "ds_read_b128 v[216:219], %3 offset:7088\n"
    "v_exp_f32 v130, v130\n"
    "v_exp_f32 v131, v131\n"
    "v_exp_f32 v132, v132\n"
    "v_exp_f32 v133, v133\n"
    "v_mfma_f32_32x32x8_bf16 v[160:175], v[200:201], v[92:93], v[236:251]\n"
    "v_exp_f32 v134, v134\n"
    "v_exp_f32 v135, v135\n"
    "v_exp_f32 v136, v136\n"
    "v_exp_f32 v137, v137\n"
    "v_mfma_f32_32x32x8_bf16 v[176:191], v[200:201], v[94:95], v[236:251]\n"
    "v_exp_f32 v138, v138\n"
    "v_exp_f32 v139, v139\n"
    "v_exp_f32 v140, v140\n"
    "v_exp_f32 v141, v141\n"
    "v_mfma_f32_32x32x16_bf16 v[160:175], v[192:195], v[84:87], v[160:175]\n"
    "ds_read_b128 v[192:195], %2 offset:2048\n"
    "v_exp_f32 v142, v142\n"
    "v_exp_f32 v143, v143\n"
    "v_exp_f32 v144, v144\n"
    "v_exp_f32 v145, v145\n"
    "v_mfma_f32_32x32x16_bf16 v[176:191], v[196:199], v[84:87], v[176:191]\n"
    "s_waitcnt lgkmcnt(2)\n"
    "ds_read_b128 v[196:199], %2 offset:3072\n"
    "v_exp_f32 v146, v146\n"
    "v_exp_f32 v147, v147\n"
    "v_exp_f32 v148, v148\n"
    "v_exp_f32 v149, v149\n"
    "v_smfmac_f32_32x32x32_bf16 v[96:111], v[204:207], v[128:135], %9\n"
    "v_exp_f32 v150, v150\n"
    "v_exp_f32 v151, v151\n"
    "v_exp_f32 v152, v152\n"
    "v_exp_f32 v153, v153\n"
    "v_smfmac_f32_32x32x32_bf16 v[96:111], v[208:211], v[136:143], %9\n"
    "s_mov_b32 %5, 20736\n"
    "s_cmp_eq_u32 %4, 6\n"
    "v_exp_f32 v154, v154\n"
    "v_exp_f32 v155, v155\n"
    "v_exp_f32 v156, v156\n"
    "v_exp_f32 v157, v157\n"
    "v_smfmac_f32_32x32x32_bf16 v[96:111], v[212:215], v[144:151], %9\n"
    "s_waitcnt lgkmcnt(0)\n"
    "s_cmov_b32 %5, -124416\n"
    "v_exp_f32 v158, v158\n"
    "v_exp_f32 v159, v159\n"
    "v_exp_f32 v160, v160\n"
    "v_exp_f32 v161, v161\n"
    "v_smfmac_f32_32x32x32_bf16 v[96:111], v[216:219], v[152:159], %9\n"
    "v_exp_f32 v162, v162\n"
    "v_exp_f32 v163, v163\n"
    "v_exp_f32 v164, v164\n"
    "v_exp_f32 v165, v165\n"
    "v_mfma_f32_32x32x8_bf16 v[128:143], v[202:203], v[88:89], v[220:235]\n"
    "v_exp_f32 v166, v166\n"
    "v_exp_f32 v167, v167\n"
    "v_exp_f32 v168, v168\n"
    "v_exp_f32 v169, v169\n"
    "v_mfma_f32_32x32x8_bf16 v[144:159], v[202:203], v[90:91], v[220:235]\n"
    "v_exp_f32 v170, v170\n"
    "v_exp_f32 v171, v171\n"
    "v_exp_f32 v172, v172\n"
    "v_exp_f32 v173, v173\n"
    "v_mfma_f32_32x32x16_bf16 v[128:143], v[192:195], v[80:83], v[128:143]\n"
    "v_exp_f32 v174, v174\n"
    "v_exp_f32 v175, v175\n"
    "v_exp_f32 v176, v176\n"
    "v_exp_f32 v177, v177\n"
    "v_mfma_f32_32x32x16_bf16 v[144:159], v[196:199], v[80:83], v[144:159]\n"
    "v_exp_f32 v178, v178\n"
    "v_exp_f32 v179, v179\n"
    "v_exp_f32 v180, v180\n"
    "v_exp_f32 v181, v181\n"
    "v_smfmac_f32_32x32x32_bf16 v[112:127], v[204:207], v[160:167], %9\n"
    "ds_read_b128 v[204:207], %3 offset:7744\n"
    "v_exp_f32 v182, v182\n"
    "v_exp_f32 v183, v183\n"
    "v_exp_f32 v184, v184\n"
    "v_exp_f32 v185, v185\n"
    "v_smfmac_f32_32x32x32_bf16 v[112:127], v[208:211], v[168:175], %9\n"
    "ds_read_b128 v[208:211], %3 offset:8400\n"
    "v_exp_f32 v186, v186\n"
    "v_exp_f32 v187, v187\n"
    "v_exp_f32 v188, v188\n"
    "v_exp_f32 v189, v189\n"
    "v_smfmac_f32_32x32x32_bf16 v[112:127], v[212:215], v[176:183], %9\n"
    "ds_read_b128 v[212:215], %3 offset:9056\n"
    "v_exp_f32 v190, v190\n"
    "v_exp_f32 v191, v191\n"
    "v_exp_f32 v128, v128\n"
    "v_exp_f32 v129, v129\n"
    "v_smfmac_f32_32x32x32_bf16 v[112:127], v[216:219], v[184:191], %9\n"
    "ds_read_b128 v[216:219], %3 offset:9712\n"
    "v_exp_f32 v130, v130\n"
    "v_exp_f32 v131, v131\n"
    "v_exp_f32 v132, v132\n"
    "v_exp_f32 v133, v133\n"
    "v_mfma_f32_32x32x8_bf16 v[160:175], v[202:203], v[92:93], v[236:251]\n"
    "v_exp_f32 v134, v134\n"
    "v_exp_f32 v135, v135\n"
    "v_exp_f32 v136, v136\n"
    "v_exp_f32 v137, v137\n"
    "v_mfma_f32_32x32x8_bf16 v[176:191], v[202:203], v[94:95], v[236:251]\n"
    "v_exp_f32 v138, v138\n"
    "v_exp_f32 v139, v139\n"
    "v_exp_f32 v140, v140\n"
    "v_exp_f32 v141, v141\n"
    "v_mfma_f32_32x32x16_bf16 v[160:175], v[192:195], v[84:87], v[160:175]\n"
    "ds_read_b128 v[192:195], %2 offset:10368\n"
    "v_exp_f32 v142, v142\n"
    "v_exp_f32 v143, v143\n"
    "v_exp_f32 v144, v144\n"
    "v_exp_f32 v145, v145\n"
    "v_mfma_f32_32x32x16_bf16 v[176:191], v[196:199], v[84:87], v[176:191]\n"
    "s_waitcnt lgkmcnt(2)\n"
    "ds_read_b128 v[196:199], %2 offset:11392\n"
    "v_exp_f32 v146, v146\n"
    "v_exp_f32 v147, v147\n"
    "v_exp_f32 v148, v148\n"
    "v_exp_f32 v149, v149\n"
    "v_smfmac_f32_32x32x32_bf16 v[96:111], v[204:207], v[128:135], %9\n"
    "ds_read_b128 v[200:203], %2 offset:14464\n"
    "v_exp_f32 v150, v150\n"
    "v_exp_f32 v151, v151\n"
    "v_exp_f32 v152, v152\n"
    "v_exp_f32 v153, v153\n"
    "v_smfmac_f32_32x32x32_bf16 v[96:111], v[208:211], v[136:143], %9\n"
    "v_exp_f32 v154, v154\n"
    "v_exp_f32 v155, v155\n"
    "v_exp_f32 v156, v156\n"
    "v_exp_f32 v157, v157\n"
    "v_smfmac_f32_32x32x32_bf16 v[96:111], v[212:215], v[144:151], %9\n"
    "s_waitcnt lgkmcnt(1)\n"
    "v_exp_f32 v158, v158\n"
    "v_exp_f32 v159, v159\n"
    "v_exp_f32 v160, v160\n"
    "v_exp_f32 v161, v161\n"
    "v_smfmac_f32_32x32x32_bf16 v[96:111], v[216:219], v[152:159], %9\n"
    "s_waitcnt lgkmcnt(0)\n"
    "v_exp_f32 v162, v162\n"
    "v_exp_f32 v163, v163\n"
    "v_exp_f32 v164, v164\n"
    "v_exp_f32 v165, v165\n"
    "v_mfma_f32_32x32x8_bf16 v[128:143], v[200:201], v[88:89], v[220:235]\n"
    "v_exp_f32 v166, v166\n"
    "v_exp_f32 v167, v167\n"
    "v_exp_f32 v168, v168\n"
    "v_exp_f32 v169, v169\n"
    "v_mfma_f32_32x32x8_bf16 v[144:159], v[200:201], v[90:91], v[220:235]\n"
    "v_exp_f32 v170, v170\n"
    "v_exp_f32 v171, v171\n"
    "v_exp_f32 v172, v172\n"
    "v_exp_f32 v173, v173\n"
    "v_mfma_f32_32x32x16_bf16 v[128:143], v[192:195], v[80:83], v[128:143]\n"
    "v_exp_f32 v174, v174\n"
    "v_exp_f32 v175, v175\n"
    "v_exp_f32 v176, v176\n"
    "v_exp_f32 v177, v177\n"
    "v_mfma_f32_32x32x16_bf16 v[144:159], v[196:199], v[80:83], v[144:159]\n"
    "v_exp_f32 v178, v178\n"
    "v_exp_f32 v179, v179\n"
    "v_exp_f32 v180, v180\n"
    "v_exp_f32 v181, v181\n"
    "v_smfmac_f32_32x32x32_bf16 v[112:127], v[204:207], v[160:167], %9\n"
    "ds_read_b128 v[204:207], %3 offset:15488\n"
    "v_exp_f32 v182, v182\n"
    "v_exp_f32 v183, v183\n"
    "v_exp_f32 v184, v184\n"
    "v_exp_f32 v185, v185\n"
    "v_smfmac_f32_32x32x32_bf16 v[112:127], v[208:211], v[168:175], %9\n"
    "ds_read_b128 v[208:211], %3 offset:16144\n"
    "v_exp_f32 v186, v186\n"
    "v_exp_f32 v187, v187\n"
    "v_exp_f32 v188, v188\n"
    "v_exp_f32 v189, v189\n"
    "v_smfmac_f32_32x32x32_bf16 v[112:127], v[212:215], v[176:183], %9\n"
    "ds_read_b128 v[212:215], %3 offset:16800\n"
    "v_exp_f32 v190, v190\n"
    "v_exp_f32 v191, v191\n"
    "v_exp_f32 v128, v128\n"
    "v_exp_f32 v129, v129\n"
    "v_smfmac_f32_32x32x32_bf16 v[112:127], v[216:219], v[184:191], %9\n"
    "ds_read_b128 v[216:219], %3 offset:17456\n"
    "v_exp_f32 v130, v130\n"
    "v_exp_f32 v131, v131\n"
    "v_exp_f32 v132, v132\n"
    "v_exp_f32 v133, v133\n"
    "v_mfma_f32_32x32x8_bf16 v[160:175], v[200:201], v[92:93], v[236:251]\n"
    "v_exp_f32 v134, v134\n"
    "v_exp_f32 v135, v135\n"
    "v_exp_f32 v136, v136\n"
    "v_exp_f32 v137, v137\n"
    "v_mfma_f32_32x32x8_bf16 v[176:191], v[200:201], v[94:95], v[236:251]\n"
    "v_exp_f32 v138, v138\n"
    "v_exp_f32 v139, v139\n"
    "v_exp_f32 v140, v140\n"
    "v_exp_f32 v141, v141\n"
    "v_mfma_f32_32x32x16_bf16 v[160:175], v[192:195], v[84:87], v[160:175]\n"
    "ds_read_b128 v[192:195], %2 offset:12416\n"
    "v_exp_f32 v142, v142\n"
    "v_exp_f32 v143, v143\n"
    "v_exp_f32 v144, v144\n"
    "v_exp_f32 v145, v145\n"
    "v_mfma_f32_32x32x16_bf16 v[176:191], v[196:199], v[84:87], v[176:191]\n"
    "s_waitcnt lgkmcnt(2)\n"
    "ds_read_b128 v[196:199], %2 offset:13440\n"
    "v_exp_f32 v146, v146\n"
    "v_exp_f32 v147, v147\n"
    "v_exp_f32 v148, v148\n"
    "v_exp_f32 v149, v149\n"
    "v_smfmac_f32_32x32x32_bf16 v[96:111], v[204:207], v[128:135], %9\n"
    "v_exp_f32 v150, v150\n"
    "v_exp_f32 v151, v151\n"
    "v_exp_f32 v152, v152\n"
    "v_exp_f32 v153, v153\n"
    "v_smfmac_f32_32x32x32_bf16 v[96:111], v[208:211], v[136:143], %9\n"
    "v_exp_f32 v154, v154\n"
    "v_exp_f32 v155, v155\n"
    "v_exp_f32 v156, v156\n"
    "v_exp_f32 v157, v157\n"
    "v_smfmac_f32_32x32x32_bf16 v[96:111], v[212:215], v[144:151], %9\n"
    "s_waitcnt lgkmcnt(0)\n"
    "v_exp_f32 v158, v158\n"
    "v_exp_f32 v159, v159\n"
    "v_exp_f32 v160, v160\n"
    "v_exp_f32 v161, v161\n"
    "v_smfmac_f32_32x32x32_bf16 v[96:111], v[216:219], v[152:159], %9\n"
    "v_exp_f32 v162, v162\n"
    "v_exp_f32 v163, v163\n"
    "v_exp_f32 v164, v164\n"
    "v_exp_f32 v165, v165\n"
    "v_mfma_f32_32x32x8_bf16 v[128:143], v[202:203], v[88:89], v[220:235]\n"
    "v_exp_f32 v166, v166\n"
    "v_exp_f32 v167, v167\n"
    "v_exp_f32 v168, v168\n"
    "v_exp_f32 v169, v169\n"
    "v_mfma_f32_32x32x8_bf16 v[144:159], v[202:203], v[90:91], v[220:235]\n"
    "v_exp_f32 v170, v170\n"
    "v_exp_f32 v171, v171\n"
    "v_exp_f32 v172, v172\n"
    "v_exp_f32 v173, v173\n"
    "v_mfma_f32_32x32x16_bf16 v[128:143], v[192:195], v[80:83], v[128:143]\n"
    "v_exp_f32 v174, v174\n"
    "v_exp_f32 v175, v175\n"
    "v_exp_f32 v176, v176\n"
    "v_exp_f32 v177, v177\n"
    "v_mfma_f32_32x32x16_bf16 v[144:159], v[196:199], v[80:83], v[144:159]\n"
    "v_exp_f32 v178, v178\n"
    "v_exp_f32 v179, v179\n"
    "v_exp_f32 v180, v180\n"
    "v_exp_f32 v181, v181\n"
    "v_smfmac_f32_32x32x32_bf16 v[112:127], v[204:207], v[160:167], %9\n"
    "ds_read_b128 v[204:207], %3 offset:18112\n"
    "v_exp_f32 v182, v182\n"
    "v_exp_f32 v183, v183\n"
    "v_exp_f32 v184, v184\n"
    "v_exp_f32 v185, v185\n"
    "v_smfmac_f32_32x32x32_bf16 v[112:127], v[208:211], v[168:175], %9\n"
    "ds_read_b128 v[208:211], %3 offset:18768\n"
    "v_exp_f32 v186, v186\n"
    "v_exp_f32 v187, v187\n"
    "v_exp_f32 v188, v188\n"
    "v_exp_f32 v189, v189\n"
    "v_smfmac_f32_32x32x32_bf16 v[112:127], v[212:215], v[176:183], %9\n"
    "ds_read_b128 v[212:215], %3 offset:19424\n"
    "v_exp_f32 v190, v190\n"
    "v_exp_f32 v191, v191\n"
    "v_exp_f32 v128, v128\n"
    "v_exp_f32 v129, v129\n"
    "v_smfmac_f32_32x32x32_bf16 v[112:127], v[216:219], v[184:191], %9\n"
    "ds_read_b128 v[216:219], %3 offset:20080\n"
    "v_add_u32 %2, %5, %2\n"
    "v_add_u32 %3, %5, %3\n"
    "v_exp_f32 v130, v130\n"
    "v_exp_f32 v131, v131\n"
    "v_exp_f32 v132, v132\n"
    "v_exp_f32 v133, v133\n"
    "v_mfma_f32_32x32x8_bf16 v[160:175], v[202:203], v[92:93], v[236:251]\n"
    "v_exp_f32 v134, v134\n"
    "v_exp_f32 v135, v135\n"
    "v_exp_f32 v136, v136\n"
    "v_exp_f32 v137, v137\n"
    "v_mfma_f32_32x32x8_bf16 v[176:191], v[202:203], v[94:95], v[236:251]\n"
    "v_exp_f32 v138, v138\n"
    "v_exp_f32 v139, v139\n"
    "v_exp_f32 v140, v140\n"
    "v_exp_f32 v141, v141\n"
    "v_mfma_f32_32x32x16_bf16 v[160:175], v[192:195], v[84:87], v[160:175]\n"
    "ds_read_b128 v[192:195], %2 offset:0\n"
    "v_exp_f32 v142, v142\n"
    "v_exp_f32 v143, v143\n"
    "v_exp_f32 v144, v144\n"
    "v_exp_f32 v145, v145\n"
    "v_mfma_f32_32x32x16_bf16 v[176:191], v[196:199], v[84:87], v[176:191]\n"
    "s_waitcnt lgkmcnt(2)\n"
    "ds_read_b128 v[196:199], %2 offset:1024\n"
    "v_exp_f32 v146, v146\n"
    "v_exp_f32 v147, v147\n"
    "v_exp_f32 v148, v148\n"
    "v_exp_f32 v149, v149\n"
    "v_smfmac_f32_32x32x32_bf16 v[96:111], v[204:207], v[128:135], %9\n"
    "ds_read_b128 v[200:203], %2 offset:4096\n"
    "v_exp_f32 v150, v150\n"
    "v_exp_f32 v151, v151\n"
    "v_exp_f32 v152, v152\n"
    "v_exp_f32 v153, v153\n"
    "v_smfmac_f32_32x32x32_bf16 v[96:111], v[208:211], v[136:143], %9\n"
    "s_add_u32 %4, %4, 1\n"
    "s_bitcmp1_b32 87, %4\n"
    "v_exp_f32 v154, v154\n"
    "v_exp_f32 v155, v155\n"
    "v_exp_f32 v156, v156\n"
    "v_exp_f32 v157, v157\n"
    "v_smfmac_f32_32x32x32_bf16 v[96:111], v[212:215], v[144:151], %9\n"
    "s_waitcnt lgkmcnt(1)\n"
    "s_cselect_b64 vcc, -1, 0\n"
    "v_exp_f32 v158, v158\n"
    "v_exp_f32 v159, v159\n"
    "v_exp_f32 v160, v160\n"
    "v_exp_f32 v161, v161\n"
    "v_smfmac_f32_32x32x32_bf16 v[96:111], v[216:219], v[152:159], %9\n"
    "s_waitcnt lgkmcnt(0)\n"
    "v_exp_f32 v162, v162\n"
    "v_exp_f32 v163, v163\n"
    "v_exp_f32 v164, v164\n"
    "v_exp_f32 v165, v165\n"
    "v_mfma_f32_32x32x8_bf16 v[128:143], v[200:201], v[88:89], v[220:235]\n"
    "v_exp_f32 v166, v166\n"
    "v_exp_f32 v167, v167\n"
    "v_exp_f32 v168, v168\n"
    "v_exp_f32 v169, v169\n"
    "v_mfma_f32_32x32x8_bf16 v[144:159], v[200:201], v[90:91], v[220:235]\n"
    "v_exp_f32 v170, v170\n"
    "v_exp_f32 v171, v171\n"
    "v_exp_f32 v172, v172\n"
    "v_exp_f32 v173, v173\n"
    "v_mfma_f32_32x32x16_bf16 v[128:143], v[192:195], v[80:83], v[128:143]\n"
    "v_exp_f32 v174, v174\n"
    "v_exp_f32 v175, v175\n"
    "v_exp_f32 v176, v176\n"
    "v_exp_f32 v177, v177\n"
    "v_mfma_f32_32x32x16_bf16 v[144:159], v[196:199], v[80:83], v[144:159]\n"
    "s_cmp_lt_u32 %4, 8\n"
    "v_exp_f32 v178, v178\n"
    "v_exp_f32 v179, v179\n"
    "v_exp_f32 v180, v180\n"
    "v_exp_f32 v181, v181\n"
    "v_smfmac_f32_32x32x32_bf16 v[112:127], v[204:207], v[160:167], %9\n"
    "ds_read_b128 v[204:207], %3 offset:5120\n"
    "v_exp_f32 v182, v182\n"
    "v_exp_f32 v183, v183\n"
    "v_exp_f32 v184, v184\n"
    "v_exp_f32 v185, v185\n"
    "v_smfmac_f32_32x32x32_bf16 v[112:127], v[208:211], v[168:175], %9\n"
    "ds_read_b128 v[208:211], %3 offset:5776\n"
    "v_exp_f32 v186, v186\n"
    "v_exp_f32 v187, v187\n"
    "v_exp_f32 v188, v188\n"
    "v_exp_f32 v189, v189\n"
    "v_smfmac_f32_32x32x32_bf16 v[112:127], v[212:215], v[176:183], %9\n"
    "ds_read_b128 v[212:215], %3 offset:6432\n"
    "v_exp_f32 v190, v190\n"
    "v_exp_f32 v191, v191\n"
    "v_exp_f32 v128, v128\n"
    "v_exp_f32 v129, v129\n"
    "v_smfmac_f32_32x32x32_bf16 v[112:127], v[216:219], v[184:191], %9\n"
    "s_cbranch_vccnz 1b\n"
    "s_cbranch_scc1 2b\n"
    "s_waitcnt lgkmcnt(0)\n"
    "s_nop 10\n"
    "v_mov_b32 v252, v104\n"
    "v_mov_b32 v253, v104\n"
    "v_mov_b32 v254, v120\n"
    "v_mov_b32 v255, v120\n"
    "v_max_f32 v250, |v96|, |v112|\n"
    "v_max_f32 v251, |v97|, |v113|\n"
    "v_permlane32_swap_b32 v252, v253\n"
    "v_permlane32_swap_b32 v254, v255\n"
    "v_max3_f32 v250, v250, |v98|, |v114|\n"
    "v_max3_f32 v251, v251, |v99|, |v115|\n"
    "v_max3_f32 v250, v250, |v100|, |v116|\n"
    "v_max3_f32 v251, v251, |v101|, |v117|\n"
    "v_max3_f32 v250, v250, |v102|, |v118|\n"
    "v_max3_f32 v251, v251, |v103|, |v119|\n"
    "v_max3_f32 v250, v250, |v104|, |v120|\n"
    "v_max3_f32 v251, v251, |v105|, |v121|\n"
    "v_max3_f32 v250, v250, |v106|, |v122|\n"
    "v_max3_f32 v251, v251, |v107|, |v123|\n"
    "v_max_f32 v250, v250, v251\n"
    "v_mul_f32 v250, 0x21800000, v250\n"
    "v_mov_b32 v252, v253\n"
    "v_mov_b32 v254, v255\n"
    "v_cmp_nlt_f32 vcc, v253, %11\n"
    "s_mov_b64 %8, vcc\n"
    "v_cmp_nlt_f32 vcc, v255, %11\n"
    "s_or_b64 %8, %8, vcc\n"
    "v_cmp_nlt_f32 vcc, v250, %11\n"
    "s_or_b64 %8, %8, vcc\n"
            : "=&{v[96:111]}"(oa), "=&{v[112:127]}"(ob), "+v"(ak), "+v"(av), "=&s"(it), "=&s"(inc), "=&{v252}"(la), "=&{v254}"(lb), "=&s"(bad)
            : "v"(0xddddddddu), "s"(c), "v"(0x1p60f), "{v[80:83]}"(a1), "{v[84:87]}"(b1), "{v[88:89]}"(a2l), "{v[90:91]}"(a2h), "{v[92:93]}"(b2l), "{v[94:95]}"(b2h)
            : "memory", "scc", "vcc", "v128", "v129", "v130", "v131", "v132", "v133", "v134", "v135", "v136", "v137", "v138", "v139", "v140", "v141", "v142", "v143", "v144", "v145", "v146", "v147", "v148", "v149", "v150", "v151", "v152", "v153", "v154", "v155", "v156", "v157", "v158", "v159", "v160", "v161", "v162", "v163", "v164", "v165", "v166", "v167", "v168", "v169", "v170", "v171", "v172", "v173", "v174", "v175", "v176", "v177", "v178", "v179", "v180", "v181", "v182", "v183", "v184", "v185", "v186", "v187", "v188", "v189", "v190", "v191", "v192", "v193", "v194", "v195", "v196", "v197", "v198", "v199", "v200", "v201", "v202", "v203", "v204", "v205", "v206", "v207", "v208", "v209", "v210", "v211", "v212", "v213", "v214", "v215", "v216", "v217", "v218", "v219", "v220", "v221", "v222", "v223", "v224", "v225", "v226", "v227", "v228", "v229", "v230", "v231", "v232", "v233", "v234", "v235", "v236", "v237", "v238", "v239", "v240", "v241", "v242", "v243", "v244", "v245", "v246", "v247", "v248", "v249", "v250", "v251", "v253", "v255");
    }
    if (__builtin_expect(bad != 0, 0)) {
        __builtin_amdgcn_fence(__ATOMIC_ACQUIRE, "agent");
#pragma unroll 1
        for (int T = 0; T < 2; ++T) {
            const bf16x8 q1 = T ? b1 : a1; const s16x4 q2 = T ? b2l : a2l;
            float m = -__builtin_inff(); f32x16 o = zero16;
#pragma unroll 1
            for (int t = 0; t < 64; ++t) {
                gptr b = (gptr)kvh + (t >> 2) * KVB; const int G = t & 3;
                f32x16 s = mf32k8(*(const GAS s16x4*)(b + 4096 + ((G & 1) * 32 + r) * 16 + (G >> 1) * 8), q2, zero16); s = mf32(*(const GAS bf16x8*)(b + G * 1024 + l * 16), q1, s);
                float tm = __builtin_fmaxf(s[0], s[1]);
#pragma unroll
                for (int i = 2; i < 16; i += 2) tm = max3f(tm, s[i], s[i + 1]);
                tm = max3f(tm, other_half(tm), m);
                const float al = __builtin_amdgcn_exp2f(m - tm); m = tm;
#pragma unroll
                for (int i = 0; i < 16; ++i) { o[i] *= al; s[i] = __builtin_amdgcn_exp2f(s[i] - m); }
#pragma unroll
                for (int u = 0; u < 2; ++u) {
                    gptr vp = b + 5120 + (2 * G + u) * 656 + (r < 20 ? r * 16 + hh * 8 : 640);
                    const u32x2 va = *(const GAS u32x2*)vp, vb = *(const GAS u32x2*)(vp + (r < 20 ? 320 : 0));
                    o = mf32(__builtin_bit_cast(bf16x8, u32x4{va[0], va[1], vb[0], vb[1]}),
                             __builtin_bit_cast(bf16x8, u32x4{pk2(s[8 * u], s[8 * u + 1]), pk2(s[8 * u + 2], s[8 * u + 3]), pk2(s[8 * u + 4], s[8 * u + 5]), pk2(s[8 * u + 6], s[8 * u + 7])}), o); }
            }
            const float lh = other_half(o[8]);
            oa = ob; la = lb; ob = o; lb = hh ? o[8] : lh;
        }
    }
    asm volatile(
    "v_rcp_f32 v252, v252\n"
    "v_rcp_f32 v254, v254\n"
    "s_nop 0\n"
    "v_pk_mul_f32 v[96:97], v[96:97], v[252:253] op_sel_hi:[1,0]\n"
    "v_pk_mul_f32 v[112:113], v[112:113], v[254:255] op_sel_hi:[1,0]\n"
    "v_pk_mul_f32 v[98:99], v[98:99], v[252:253] op_sel_hi:[1,0]\n"
    "v_pk_mul_f32 v[114:115], v[114:115], v[254:255] op_sel_hi:[1,0]\n"
    "v_pk_mul_f32 v[100:101], v[100:101], v[252:253] op_sel_hi:[1,0]\n"
    "v_pk_mul_f32 v[116:117], v[116:117], v[254:255] op_sel_hi:[1,0]\n"
    "v_pk_mul_f32 v[102:103], v[102:103], v[252:253] op_sel_hi:[1,0]\n"
    "v_pk_mul_f32 v[118:119], v[118:119], v[254:255] op_sel_hi:[1,0]\n"
    "v_pk_mul_f32 v[104:105], v[104:105], v[252:253] op_sel_hi:[1,0]\n"
    "v_pk_mul_f32 v[120:121], v[120:121], v[254:255] op_sel_hi:[1,0]\n"
    "v_pk_mul_f32 v[106:107], v[106:107], v[252:253] op_sel_hi:[1,0]\n"
    "v_pk_mul_f32 v[122:123], v[122:123], v[254:255] op_sel_hi:[1,0]\n"
    "v_cvt_pk_bf16_f32 v224, v96, v97\n"
    "v_cvt_pk_bf16_f32 v232, v112, v113\n"
    "v_cvt_pk_bf16_f32 v226, v100, v101\n"
    "v_cvt_pk_bf16_f32 v234, v116, v117\n"
    "v_cvt_pk_bf16_f32 v225, v98, v99\n"
    "v_cvt_pk_bf16_f32 v233, v114, v115\n"
    "v_cvt_pk_bf16_f32 v227, v102, v103\n"
    "v_cvt_pk_bf16_f32 v235, v118, v119\n"
    "v_cvt_pk_bf16_f32 v228, v104, v105\n"
    "v_cvt_pk_bf16_f32 v236, v120, v121\n"
    "v_cvt_pk_bf16_f32 v229, v106, v107\n"
    "v_cvt_pk_bf16_f32 v237, v122, v123\n"
    "v_permlane32_swap_b32 v224, v226\n"
    "v_permlane32_swap_b32 v232, v234\n"
    "v_permlane32_swap_b32 v225, v227\n"
    "v_permlane32_swap_b32 v233, v235\n"
    "v_add_u32 v230, 40960, %5\n"
    "v_add_u32 v231, 40960, %6\n"
    "global_store_dwordx4 %5, v[224:227], %4 sc1\n"
    "global_store_dwordx4 v230, v[232:235], %4 sc1\n"
    "s_mov_b32 exec_hi, 0\n"
    "global_store_dwordx2 %6, v[228:229], %4 offset:1024 sc1\n"
    "global_store_dwordx2 v231, v[236:237], %4 offset:1024 sc1\n"
    "s_mov_b32 exec_hi, -1\n"
        : "+{v[96:111]}"(oa), "+{v[112:127]}"(ob), "+{v252}"(la), "+{v254}"(lb) : "s"(ws + WS_O + 2621440 + (size_t)tt * 40960 + h * 1280), "v"(l * 16), "v"(r * 8) : "memory", "v224", "v225", "v226", "v227", "v228", "v229", "v230", "v231", "v232", "v233", "v234", "v235", "v236", "v237", "v238", "v239");
    }
    asm volatile("s_waitcnt vmcnt(0) lgkmcnt(0)\n\ts_barrier" ::: "memory");
    LANES(); r = l & 31;
    const int t3 = (xcd & 3) * 4 + (slot >> 3);
    if (w == 0 && l == 0) post(ctl, CTL_FO + ((blockIdx.x >> 5) * 32 + h) * FS, tag);
    unsigned aw = ldsb + K3_WO + l * 16, v4 = (g >> 1) * 1280 + (g & 1) * 512 + tau * 16, v2 = g * 2560 + 1024 + tau * 8;
    const char* og = ws + WS_O + (size_t)(xcd >> 2) * 2621440 + (size_t)(t3 * 4 + c) * 40960;
    asm volatile("" : "+s"(og)); asm volatile("" : "+v"(aw), "+v"(v4), "+v"(v2));
    if (w == 5) await(ctl, CTL_FO + ((t3 >> 1) * 32 + r) * FS, tag);
    asm volatile("s_barrier" ::: "memory");
    if (w < 4) {
        f32x16 A0, A1; f32x8 A2;
        asm volatile(
    "global_load_dwordx4 v[96:99], %4, %6 offset:0 sc1\n"
    "global_load_dwordx4 v[100:103], %4, %6 offset:256 sc1\n"
    "global_load_dwordx4 v[104:107], %4, %6 offset:2560 sc1\n"
    "global_load_dwordx4 v[108:111], %4, %6 offset:2816 sc1\n"
    "v_add_u32 %4, 5120, %4\n"
    "global_load_dwordx4 v[112:115], %4, %6 offset:0 sc1\n"
    "global_load_dwordx4 v[116:119], %4, %6 offset:256 sc1\n"
    "global_load_dwordx4 v[120:123], %4, %6 offset:2560 sc1\n"
    "global_load_dwordx4 v[124:127], %4, %6 offset:2816 sc1\n"
    "v_add_u32 %4, 5120, %4\n"
    "global_load_dwordx4 v[128:131], %4, %6 offset:0 sc1\n"
    "global_load_dwordx4 v[132:135], %4, %6 offset:256 sc1\n"
    "ds_read_b128 v[196:199], %3 offset:0\n"
    "ds_read_b128 v[200:203], %3 offset:1024\n"
    "ds_read_b128 v[204:207], %3 offset:2048\n"
    "ds_read_b128 v[208:211], %3 offset:3072\n"
    "ds_read_b128 v[212:215], %3 offset:4096\n"
    "s_waitcnt vmcnt(8) lgkmcnt(3)\n"
    "v_mfma_f32_16x16x32_bf16 v[56:59], v[196:199], v[96:99], 0\n"
    "ds_read_b128 v[216:219], %3 offset:5120\n"
    "v_mfma_f32_16x16x32_bf16 v[60:63], v[196:199], v[100:103], 0\n"
    "global_load_dwordx4 v[136:139], %4, %6 offset:2560 sc1\n"
    "v_mfma_f32_16x16x32_bf16 v[64:67], v[200:203], v[96:99], 0\n"
    "ds_read_b128 v[220:223], %3 offset:6144\n"
    "v_mfma_f32_16x16x32_bf16 v[68:71], v[200:203], v[100:103], 0\n"
    "global_load_dwordx4 v[140:143], %4, %6 offset:2816 sc1\n"
    "s_waitcnt lgkmcnt(2)\n"
    "v_mfma_f32_16x16x32_bf16 v[72:75], v[204:207], v[96:99], 0\n"
    "ds_read_b128 v[224:227], %3 offset:7168\n"
    "v_mfma_f32_16x16x32_bf16 v[76:79], v[204:207], v[100:103], 0\n"
    "v_add_u32 %4, 5120, %4\n"
    "v_mfma_f32_16x16x32_bf16 v[80:83], v[208:211], v[96:99], 0\n"
    "ds_read_b128 v[228:231], %3 offset:8192\n"
    "v_mfma_f32_16x16x32_bf16 v[84:87], v[208:211], v[100:103], 0\n"
    "v_mfma_f32_16x16x32_bf16 v[88:91], v[212:215], v[96:99], 0\n"
    "ds_read_b128 v[232:235], %3 offset:9216\n"
    "v_mfma_f32_16x16x32_bf16 v[92:95], v[212:215], v[100:103], 0\n"
    "s_waitcnt vmcnt(8) lgkmcnt(3)\n"
    "v_mfma_f32_16x16x32_bf16 v[56:59], v[216:219], v[104:107], v[56:59]\n"
    "ds_read_b128 v[196:199], %3 offset:10240\n"
    "v_mfma_f32_16x16x32_bf16 v[60:63], v[216:219], v[108:111], v[60:63]\n"
    "global_load_dwordx4 v[144:147], %4, %6 offset:0 sc1\n"
    "v_mfma_f32_16x16x32_bf16 v[64:67], v[220:223], v[104:107], v[64:67]\n"
    "ds_read_b128 v[200:203], %3 offset:11264\n"
    "v_mfma_f32_16x16x32_bf16 v[68:71], v[220:223], v[108:111], v[68:71]\n"
    "global_load_dwordx4 v[148:151], %4, %6 offset:256 sc1\n"
    "s_waitcnt lgkmcnt(2)\n"
    "v_mfma_f32_16x16x32_bf16 v[72:75], v[224:227], v[104:107], v[72:75]\n"
    "ds_read_b128 v[204:207], %3 offset:12288\n"
    "v_mfma_f32_16x16x32_bf16 v[76:79], v[224:227], v[108:111], v[76:79]\n"
    "v_mfma_f32_16x16x32_bf16 v[80:83], v[228:231], v[104:107], v[80:83]\n"
    "ds_read_b128 v[208:211], %3 offset:13312\n"
    "v_mfma_f32_16x16x32_bf16 v[84:87], v[228:231], v[108:111], v[84:87]\n"
    "v_mfma_f32_16x16x32_bf16 v[88:91], v[232:235], v[104:107], v[88:91]\n"
    "ds_read_b128 v[212:215], %3 offset:14336\n"
    "v_mfma_f32_16x16x32_bf16 v[92:95], v[232:235], v[108:111], v[92:95]\n"
    "s_waitcnt vmcnt(8) lgkmcnt(3)\n"
    "v_mfma_f32_16x16x32_bf16 v[56:59], v[196:199], v[112:115], v[56:59]\n"
    "ds_read_b128 v[216:219], %3 offset:15360\n"
    "v_mfma_f32_16x16x32_bf16 v[60:63], v[196:199], v[116:119], v[60:63]\n"
    "global_load_dwordx4 v[152:155], %4, %6 offset:2560 sc1\n"
    "v_mfma_f32_16x16x32_bf16 v[64:67], v[200:203], v[112:115], v[64:67]\n"
    "ds_read_b128 v[220:223], %3 offset:16384\n"
    "v_mfma_f32_16x16x32_bf16 v[68:71], v[200:203], v[116:119], v[68:71]\n"
    "global_load_dwordx4 v[156:159], %4, %6 offset:2816 sc1\n"
    "s_waitcnt lgkmcnt(2)\n"
    "v_mfma_f32_16x16x32_bf16 v[72:75], v[204:207], v[112:115], v[72:75]\n"
    "ds_read_b128 v[224:227], %3 offset:17408\n"
    "v_mfma_f32_16x16x32_bf16 v[76:79], v[204:207], v[116:119], v[76:79]\n"
    "v_add_u32 %4, 5120, %4\n"
    "v_mfma_f32_16x16x32_bf16 v[80:83], v[208:211], v[112:115], v[80:83]\n"
    "ds_read_b128 v[228:231], %3 offset:18432\n"
    "v_mfma_f32_16x16x32_bf16 v[84:87], v[208:211], v[116:119], v[84:87]\n"
    "v_mfma_f32_16x16x32_bf16 v[88:91], v[212:215], v[112:115], v[88:91]\n"
    "ds_read_b128 v[232:235], %3 offset:19456\n"
    "v_mfma_f32_16x16x32_bf16 v[92:95], v[212:215], v[116:119], v[92:95]\n"
    "s_waitcnt vmcnt(8) lgkmcnt(3)\n"
    "v_mfma_f32_16x16x32_bf16 v[56:59], v[216:219], v[120:123], v[56:59]\n"
    "ds_read_b128 v[196:199], %3 offset:20480\n"
    "v_mfma_f32_16x16x32_bf16 v[60:63], v[216:219], v[124:127], v[60:63]\n"
    "global_load_dwordx4 v[160:163], %4, %6 offset:0 sc1\n"
    "v_mfma_f32_16x16x32_bf16 v[64:67], v[220:223], v[120:123], v[64:67]\n"
    "ds_read_b128 v[200:203], %3 offset:21504\n"
    "v_mfma_f32_16x16x32_bf16 v[68:71], v[220:223], v[124:127], v[68:71]\n"
    "global_load_dwordx4 v[164:167], %4, %6 offset:256 sc1\n"
    "s_waitcnt lgkmcnt(2)\n"
    "v_mfma_f32_16x16x32_bf16 v[72:75], v[224:227], v[120:123], v[72:75]\n"
    "ds_read_b128 v[204:207], %3 offset:22528\n"
    "v_mfma_f32_16x16x32_bf16 v[76:79], v[224:227], v[124:127], v[76:79]\n"
    "v_mfma_f32_16x16x32_bf16 v[80:83], v[228:231], v[120:123], v[80:83]\n"
    "ds_read_b128 v[208:211], %3 offset:23552\n"
    "v_mfma_f32_16x16x32_bf16 v[84:87], v[228:231], v[124:127], v[84:87]\n"
    "v_mfma_f32_16x16x32_bf16 v[88:91], v[232:235], v[120:123], v[88:91]\n"
    "ds_read_b128 v[212:215], %3 offset:24576\n"
    "v_mfma_f32_16x16x32_bf16 v[92:95], v[232:235], v[124:127], v[92:95]\n"
    "s_waitcnt vmcnt(8) lgkmcnt(3)\n"
    "v_mfma_f32_16x16x32_bf16 v[56:59], v[196:199], v[128:131], v[56:59]\n"
    "ds_read_b128 v[216:219], %3 offset:25600\n"
    "v_mfma_f32_16x16x32_bf16 v[60:63], v[196:199], v[132:135], v[60:63]\n"
    "global_load_dwordx4 v[168:171], %4, %6 offset:2560 sc1\n"
    "v_mfma_f32_16x16x32_bf16 v[64:67], v[200:203], v[128:131], v[64:67]\n"
    "ds_read_b128 v[220:223], %3 offset:26624\n"
    "v_mfma_f32_16x16x32_bf16 v[68:71], v[200:203], v[132:135], v[68:71]\n"
    "global_load_dwordx4 v[172:175], %4, %6 offset:2816 sc1\n"
    "s_waitcnt lgkmcnt(2)\n"
    "v_mfma_f32_16x16x32_bf16 v[72:75], v[204:207], v[128:131], v[72:75]\n"
    "ds_read_b128 v[224:227], %3 offset:27648\n"
    "v_mfma_f32_16x16x32_bf16 v[76:79], v[204:207], v[132:135], v[76:79]\n"
    "v_add_u32 %4, 5120, %4\n"
    "v_mfma_f32_16x16x32_bf16 v[80:83], v[208:211], v[128:131], v[80:83]\n"
    "ds_read_b128 v[228:231], %3 offset:28672\n"
    "v_mfma_f32_16x16x32_bf16 v[84:87], v[208:211], v[132:135], v[84:87]\n"
    "v_mfma_f32_16x16x32_bf16 v[88:91], v[212:215], v[128:131], v[88:91]\n"
    "ds_read_b128 v[232:235], %3 offset:29696\n"
    "v_mfma_f32_16x16x32_bf16 v[92:95], v[212:215], v[132:135], v[92:95]\n"
    "s_waitcnt vmcnt(8) lgkmcnt(3)\n"
    "v_mfma_f32_16x16x32_bf16 v[56:59], v[216:219], v[136:139], v[56:59]\n"
    "ds_read_b128 v[196:199], %3 offset:30720\n"
    "v_mfma_f32_16x16x32_bf16 v[60:63], v[216:219], v[140:143], v[60:63]\n"
    "global_load_dwordx4 v[176:179], %4, %6 offset:0 sc1\n"
    "v_mfma_f32_16x16x32_bf16 v[64:67], v[220:223], v[136:139], v[64:67]\n"
    "ds_read_b128 v[200:203], %3 offset:31744\n"
    "v_mfma_f32_16x16x32_bf16 v[68:71], v[220:223], v[140:143], v[68:71]\n"
    "global_load_dwordx4 v[180:183], %4, %6 offset:256 sc1\n"
    "s_waitcnt lgkmcnt(2)\n"
    "v_mfma_f32_16x16x32_bf16 v[72:75], v[224:227], v[136:139], v[72:75]\n"
    "ds_read_b128 v[204:207], %3 offset:32768\n"
    "v_mfma_f32_16x16x32_bf16 v[76:79], v[224:227], v[140:143], v[76:79]\n"
    "v_mfma_f32_16x16x32_bf16 v[80:83], v[228:231], v[136:139], v[80:83]\n"
    "ds_read_b128 v[208:211], %3 offset:33792\n"
    "v_mfma_f32_16x16x32_bf16 v[84:87], v[228:231], v[140:143], v[84:87]\n"
    "v_mfma_f32_16x16x32_bf16 v[88:91], v[232:235], v[136:139], v[88:91]\n"
    "ds_read_b128 v[212:215], %3 offset:34816\n"
    "v_mfma_f32_16x16x32_bf16 v[92:95], v[232:235], v[140:143], v[92:95]\n"
    "s_waitcnt vmcnt(8) lgkmcnt(3)\n"
    "v_mfma_f32_16x16x32_bf16 v[56:59], v[196:199], v[144:147], v[56:59]\n"
    "ds_read_b128 v[216:219], %3 offset:35840\n"
    "v_mfma_f32_16x16x32_bf16 v[60:63], v[196:199], v[148:151], v[60:63]\n"
    "global_load_dwordx4 v[184:187], %4, %6 offset:2560 sc1\n"
    "v_mfma_f32_16x16x32_bf16 v[64:67], v[200:203], v[144:147], v[64:67]\n"
    "ds_read_b128 v[220:223], %3 offset:36864\n"
    "v_mfma_f32_16x16x32_bf16 v[68:71], v[200:203], v[148:151], v[68:71]\n"
    "global_load_dwordx4 v[188:191], %4, %6 offset:2816 sc1\n"
    "s_waitcnt lgkmcnt(2)\n"
    "v_mfma_f32_16x16x32_bf16 v[72:75], v[204:207], v[144:147], v[72:75]\n"
    "ds_read_b128 v[224:227], %3 offset:37888\n"
    "v_mfma_f32_16x16x32_bf16 v[76:79], v[204:207], v[148:151], v[76:79]\n"
    "v_add_u32 %4, 5120, %4\n"
    "v_mfma_f32_16x16x32_bf16 v[80:83], v[208:211], v[144:147], v[80:83]\n"
    "ds_read_b128 v[228:231], %3 offset:38912\n"
    "v_mfma_f32_16x16x32_bf16 v[84:87], v[208:211], v[148:151], v[84:87]\n"
    "v_mfma_f32_16x16x32_bf16 v[88:91], v[212:215], v[144:147], v[88:91]\n"
    "ds_read_b128 v[232:235], %3 offset:39936\n"
    "v_mfma_f32_16x16x32_bf16 v[92:95], v[212:215], v[148:151], v[92:95]\n"
    "s_waitcnt vmcnt(8) lgkmcnt(3)\n"
    "v_mfma_f32_16x16x32_bf16 v[56:59], v[216:219], v[152:155], v[56:59]\n"
    "ds_read_b128 v[196:199], %3 offset:40960\n"
    "v_mfma_f32_16x16x32_bf16 v[60:63], v[216:219], v[156:159], v[60:63]\n"
    "global_load_dwordx4 v[96:99], %4, %6 offset:0 sc1\n"
    "v_mfma_f32_16x16x32_bf16 v[64:67], v[220:223], v[152:155], v[64:67]\n"
    "ds_read_b128 v[200:203], %3 offset:41984\n"
    "v_mfma_f32_16x16x32_bf16 v[68:71], v[220:223], v[156:159], v[68:71]\n"
    "global_load_dwordx4 v[100:103], %4, %6 offset:256 sc1\n"
    "s_waitcnt lgkmcnt(2)\n"
    "v_mfma_f32_16x16x32_bf16 v[72:75], v[224:227], v[152:155], v[72:75]\n"
    "ds_read_b128 v[204:207], %3 offset:43008\n"
    "v_mfma_f32_16x16x32_bf16 v[76:79], v[224:227], v[156:159], v[76:79]\n"
    "v_mfma_f32_16x16x32_bf16 v[80:83], v[228:231], v[152:155], v[80:83]\n"
    "ds_read_b128 v[208:211], %3 offset:44032\n"
    "v_mfma_f32_16x16x32_bf16 v[84:87], v[228:231], v[156:159], v[84:87]\n"
    "v_mfma_f32_16x16x32_bf16 v[88:91], v[232:235], v[152:155], v[88:91]\n"
    "ds_read_b128 v[212:215], %3 offset:45056\n"
    "v_mfma_f32_16x16x32_bf16 v[92:95], v[232:235], v[156:159], v[92:95]\n"
    "s_waitcnt vmcnt(8) lgkmcnt(3)\n"
    "v_mfma_f32_16x16x32_bf16 v[56:59], v[196:199], v[160:163], v[56:59]\n"
    "ds_read_b128 v[216:219], %3 offset:46080\n"
    "v_mfma_f32_16x16x32_bf16 v[60:63], v[196:199], v[164:167], v[60:63]\n"
    "global_load_dwordx4 v[104:107], %4, %6 offset:2560 sc1\n"
    "v_mfma_f32_16x16x32_bf16 v[64:67], v[200:203], v[160:163], v[64:67]\n"
    "ds_read_b128 v[220:223], %3 offset:47104\n"
    "v_mfma_f32_16x16x32_bf16 v[68:71], v[200:203], v[164:167], v[68:71]\n"
    "global_load_dwordx4 v[108:111], %4, %6 offset:2816 sc1\n"
    "s_waitcnt lgkmcnt(2)\n"
    "v_mfma_f32_16x16x32_bf16 v[72:75], v[204:207], v[160:163], v[72:75]\n"
    "ds_read_b128 v[224:227], %3 offset:48128\n"
    "v_mfma_f32_16x16x32_bf16 v[76:79], v[204:207], v[164:167], v[76:79]\n"
    "v_add_u32 %4, 5120, %4\n"
    "v_mfma_f32_16x16x32_bf16 v[80:83], v[208:211], v[160:163], v[80:83]\n"
    "ds_read_b128 v[228:231], %3 offset:49152\n"
    "v_mfma_f32_16x16x32_bf16 v[84:87], v[208:211], v[164:167], v[84:87]\n"
    "v_mfma_f32_16x16x32_bf16 v[88:91], v[212:215], v[160:163], v[88:91]\n"
    "ds_read_b128 v[232:235], %3 offset:50176\n"
    "v_mfma_f32_16x16x32_bf16 v[92:95], v[212:215], v[164:167], v[92:95]\n"
    "s_waitcnt vmcnt(8) lgkmcnt(3)\n"
    "v_mfma_f32_16x16x32_bf16 v[56:59], v[216:219], v[168:171], v[56:59]\n"
    "ds_read_b128 v[196:199], %3 offset:51200\n"
    "v_mfma_f32_16x16x32_bf16 v[60:63], v[216:219], v[172:175], v[60:63]\n"
    "global_load_dwordx4 v[112:115], %4, %6 offset:0 sc1\n"
    "v_mfma_f32_16x16x32_bf16 v[64:67], v[220:223], v[168:171], v[64:67]\n"
    "ds_read_b128 v[200:203], %3 offset:52224\n"
    "v_mfma_f32_16x16x32_bf16 v[68:71], v[220:223], v[172:175], v[68:71]\n"
    "global_load_dwordx4 v[116:119], %4, %6 offset:256 sc1\n"
    "s_waitcnt lgkmcnt(2)\n"
    "v_mfma_f32_16x16x32_bf16 v[72:75], v[224:227], v[168:171], v[72:75]\n"
    "ds_read_b128 v[204:207], %3 offset:53248\n"
    "v_mfma_f32_16x16x32_bf16 v[76:79], v[224:227], v[172:175], v[76:79]\n"
    "v_mfma_f32_16x16x32_bf16 v[80:83], v[228:231], v[168:171], v[80:83]\n"
    "ds_read_b128 v[208:211], %3 offset:54272\n"
    "v_mfma_f32_16x16x32_bf16 v[84:87], v[228:231], v[172:175], v[84:87]\n"
    "v_mfma_f32_16x16x32_bf16 v[88:91], v[232:235], v[168:171], v[88:91]\n"
    "ds_read_b128 v[212:215], %3 offset:55296\n"
    "v_mfma_f32_16x16x32_bf16 v[92:95], v[232:235], v[172:175], v[92:95]\n"
    "s_waitcnt vmcnt(8) lgkmcnt(3)\n"
    "v_mfma_f32_16x16x32_bf16 v[56:59], v[196:199], v[176:179], v[56:59]\n"
    "ds_read_b128 v[216:219], %3 offset:56320\n"
    "v_mfma_f32_16x16x32_bf16 v[60:63], v[196:199], v[180:183], v[60:63]\n"
    "global_load_dwordx4 v[120:123], %4, %6 offset:2560 sc1\n"
    "v_mfma_f32_16x16x32_bf16 v[64:67], v[200:203], v[176:179], v[64:67]\n"
    "ds_read_b128 v[220:223], %3 offset:57344\n"
    "v_mfma_f32_16x16x32_bf16 v[68:71], v[200:203], v[180:183], v[68:71]\n"
    "global_load_dwordx4 v[124:127], %4, %6 offset:2816 sc1\n"
    "s_waitcnt lgkmcnt(2)\n"
    "v_mfma_f32_16x16x32_bf16 v[72:75], v[204:207], v[176:179], v[72:75]\n"
    "ds_read_b128 v[224:227], %3 offset:58368\n"
    "v_mfma_f32_16x16x32_bf16 v[76:79], v[204:207], v[180:183], v[76:79]\n"
    "v_mfma_f32_16x16x32_bf16 v[80:83], v[208:211], v[176:179], v[80:83]\n"
    "ds_read_b128 v[228:231], %3 offset:59392\n"
    "v_mfma_f32_16x16x32_bf16 v[84:87], v[208:211], v[180:183], v[84:87]\n"
    "v_mfma_f32_16x16x32_bf16 v[88:91], v[212:215], v[176:179], v[88:91]\n"
    "ds_read_b128 v[232:235], %3 offset:60416\n"
    "v_mfma_f32_16x16x32_bf16 v[92:95], v[212:215], v[180:183], v[92:95]\n"
    "v_add_u32 %3, 61440, %3\n"
    "s_waitcnt vmcnt(8) lgkmcnt(3)\n"
    "v_mfma_f32_16x16x32_bf16 v[56:59], v[216:219], v[184:187], v[56:59]\n"
    "ds_read_b128 v[196:199], %3 offset:0\n"
    "v_mfma_f32_16x16x32_bf16 v[60:63], v[216:219], v[188:191], v[60:63]\n"
    "global_load_dwordx2 v[128:129], %5, %6 offset:0 sc1\n"
    "v_mfma_f32_16x16x32_bf16 v[64:67], v[220:223], v[184:187], v[64:67]\n"
    "ds_read_b128 v[200:203], %3 offset:1024\n"
    "v_mfma_f32_16x16x32_bf16 v[68:71], v[220:223], v[188:191], v[68:71]\n"
    "global_load_dwordx2 v[130:131], %5, %6 offset:1280 sc1\n"
    "s_waitcnt lgkmcnt(2)\n"
    "v_mfma_f32_16x16x32_bf16 v[72:75], v[224:227], v[184:187], v[72:75]\n"
    "ds_read_b128 v[204:207], %3 offset:2048\n"
    "v_mfma_f32_16x16x32_bf16 v[76:79], v[224:227], v[188:191], v[76:79]\n"
    "global_load_dwordx2 v[132:133], %5, %6 offset:128 sc1\n"
    "v_mfma_f32_16x16x32_bf16 v[80:83], v[228:231], v[184:187], v[80:83]\n"
    "ds_read_b128 v[208:211], %3 offset:3072\n"
    "v_mfma_f32_16x16x32_bf16 v[84:87], v[228:231], v[188:191], v[84:87]\n"
    "global_load_dwordx2 v[134:135], %5, %6 offset:1408 sc1\n"
    "v_mfma_f32_16x16x32_bf16 v[88:91], v[232:235], v[184:187], v[88:91]\n"
    "ds_read_b128 v[212:215], %3 offset:4096\n"
    "v_mfma_f32_16x16x32_bf16 v[92:95], v[232:235], v[188:191], v[92:95]\n"
    "v_add_u32 %5, 10240, %5\n"
    "s_waitcnt vmcnt(10) lgkmcnt(3)\n"
    "v_mfma_f32_16x16x32_bf16 v[56:59], v[196:199], v[96:99], v[56:59]\n"
    "ds_read_b128 v[216:219], %3 offset:5120\n"
    "v_mfma_f32_16x16x32_bf16 v[60:63], v[196:199], v[100:103], v[60:63]\n"
    "global_load_dwordx2 v[136:137], %5, %6 offset:0 sc1\n"
    "v_mfma_f32_16x16x32_bf16 v[64:67], v[200:203], v[96:99], v[64:67]\n"
    "ds_read_b128 v[220:223], %3 offset:6144\n"
    "v_mfma_f32_16x16x32_bf16 v[68:71], v[200:203], v[100:103], v[68:71]\n"
    "global_load_dwordx2 v[138:139], %5, %6 offset:1280 sc1\n"
    "s_waitcnt lgkmcnt(2)\n"
    "v_mfma_f32_16x16x32_bf16 v[72:75], v[204:207], v[96:99], v[72:75]\n"
    "ds_read_b128 v[224:227], %3 offset:7168\n"
    "v_mfma_f32_16x16x32_bf16 v[76:79], v[204:207], v[100:103], v[76:79]\n"
    "global_load_dwordx2 v[140:141], %5, %6 offset:128 sc1\n"
    "v_mfma_f32_16x16x32_bf16 v[80:83], v[208:211], v[96:99], v[80:83]\n"
    "ds_read_b128 v[228:231], %3 offset:8192\n"
    "v_mfma_f32_16x16x32_bf16 v[84:87], v[208:211], v[100:103], v[84:87]\n"
    "global_load_dwordx2 v[142:143], %5, %6 offset:1408 sc1\n"
    "v_mfma_f32_16x16x32_bf16 v[88:91], v[212:215], v[96:99], v[88:91]\n"
    "ds_read_b128 v[232:235], %3 offset:9216\n"
    "v_mfma_f32_16x16x32_bf16 v[92:95], v[212:215], v[100:103], v[92:95]\n"
    "v_add_u32 %5, 10240, %5\n"
    "s_waitcnt vmcnt(12) lgkmcnt(3)\n"
    "v_mfma_f32_16x16x32_bf16 v[56:59], v[216:219], v[104:107], v[56:59]\n"
    "ds_read_b128 v[196:199], %3 offset:10240\n"
    "v_mfma_f32_16x16x32_bf16 v[60:63], v[216:219], v[108:111], v[60:63]\n"
    "global_load_dwordx2 v[144:145], %5, %6 offset:0 sc1\n"
    "v_mfma_f32_16x16x32_bf16 v[64:67], v[220:223], v[104:107], v[64:67]\n"
    "ds_read_b128 v[200:203], %3 offset:11264\n"
    "v_mfma_f32_16x16x32_bf16 v[68:71], v[220:223], v[108:111], v[68:71]\n"
    "global_load_dwordx2 v[146:147], %5, %6 offset:1280 sc1\n"
    "s_waitcnt lgkmcnt(2)\n"
    "v_mfma_f32_16x16x32_bf16 v[72:75], v[224:227], v[104:107], v[72:75]\n"
    "ds_read_b128 v[204:207], %3 offset:12288\n"
    "v_mfma_f32_16x16x32_bf16 v[76:79], v[224:227], v[108:111], v[76:79]\n"
    "global_load_dwordx2 v[148:149], %5, %6 offset:128 sc1\n"
    "v_mfma_f32_16x16x32_bf16 v[80:83], v[228:231], v[104:107], v[80:83]\n"
    "ds_read_b128 v[208:211], %3 offset:13312\n"
    "v_mfma_f32_16x16x32_bf16 v[84:87], v[228:231], v[108:111], v[84:87]\n"
    "global_load_dwordx2 v[150:151], %5, %6 offset:1408 sc1\n"
    "v_mfma_f32_16x16x32_bf16 v[88:91], v[232:235], v[104:107], v[88:91]\n"
    "ds_read_b128 v[212:215], %3 offset:14336\n"
    "v_mfma_f32_16x16x32_bf16 v[92:95], v[232:235], v[108:111], v[92:95]\n"
    "v_add_u32 %5, 10240, %5\n"
    "s_waitcnt vmcnt(14) lgkmcnt(3)\n"
    "v_mfma_f32_16x16x32_bf16 v[56:59], v[196:199], v[112:115], v[56:59]\n"
    "ds_read_b128 v[216:219], %3 offset:15360\n"
    "v_mfma_f32_16x16x32_bf16 v[60:63], v[196:199], v[116:119], v[60:63]\n"
    "global_load_dwordx2 v[152:153], %5, %6 offset:0 sc1\n"
    "v_mfma_f32_16x16x32_bf16 v[64:67], v[200:203], v[112:115], v[64:67]\n"
    "ds_read_b128 v[220:223], %3 offset:16384\n"
    "v_mfma_f32_16x16x32_bf16 v[68:71], v[200:203], v[116:119], v[68:71]\n"
    "global_load_dwordx2 v[154:155], %5, %6 offset:1280 sc1\n"
    "s_waitcnt lgkmcnt(2)\n"
    "v_mfma_f32_16x16x32_bf16 v[72:75], v[204:207], v[112:115], v[72:75]\n"
    "ds_read_b128 v[224:227], %3 offset:17408\n"
    "v_mfma_f32_16x16x32_bf16 v[76:79], v[204:207], v[116:119], v[76:79]\n"
    "global_load_dwordx2 v[156:157], %5, %6 offset:128 sc1\n"
    "v_mfma_f32_16x16x32_bf16 v[80:83], v[208:211], v[112:115], v[80:83]\n"
    "ds_read_b128 v[228:231], %3 offset:18432\n"
    "v_mfma_f32_16x16x32_bf16 v[84:87], v[208:211], v[116:119], v[84:87]\n"
    "global_load_dwordx2 v[158:159], %5, %6 offset:1408 sc1\n"
    "v_mfma_f32_16x16x32_bf16 v[88:91], v[212:215], v[112:115], v[88:91]\n"
    "ds_read_b128 v[232:235], %3 offset:19456\n"
    "v_mfma_f32_16x16x32_bf16 v[92:95], v[212:215], v[116:119], v[92:95]\n"
    "s_waitcnt vmcnt(16) lgkmcnt(3)\n"
    "v_mfma_f32_16x16x32_bf16 v[56:59], v[216:219], v[120:123], v[56:59]\n"
    "ds_read_b128 v[196:199], %3 offset:20480\n"
    "v_mfma_f32_16x16x32_bf16 v[60:63], v[216:219], v[124:127], v[60:63]\n"
    "v_mfma_f32_16x16x32_bf16 v[64:67], v[220:223], v[120:123], v[64:67]\n"
    "ds_read_b128 v[200:203], %3 offset:21504\n"
    "v_mfma_f32_16x16x32_bf16 v[68:71], v[220:223], v[124:127], v[68:71]\n"
    "s_waitcnt lgkmcnt(2)\n"
    "v_mfma_f32_16x16x32_bf16 v[72:75], v[224:227], v[120:123], v[72:75]\n"
    "ds_read_b128 v[204:207], %3 offset:22528\n"
    "v_mfma_f32_16x16x32_bf16 v[76:79], v[224:227], v[124:127], v[76:79]\n"
    "v_mfma_f32_16x16x32_bf16 v[80:83], v[228:231], v[120:123], v[80:83]\n"
    "ds_read_b128 v[208:211], %3 offset:23552\n"
    "v_mfma_f32_16x16x32_bf16 v[84:87], v[228:231], v[124:127], v[84:87]\n"
    "v_mfma_f32_16x16x32_bf16 v[88:91], v[232:235], v[120:123], v[88:91]\n"
    "ds_read_b128 v[212:215], %3 offset:24576\n"
    "v_mfma_f32_16x16x32_bf16 v[92:95], v[232:235], v[124:127], v[92:95]\n"
    "s_waitcnt vmcnt(12) lgkmcnt(3)\n"
    "v_mfma_f32_16x16x32_bf16 v[56:59], v[196:199], v[128:131], v[56:59]\n"
    "ds_read_b128 v[216:219], %3 offset:25600\n"
    "v_mfma_f32_16x16x32_bf16 v[60:63], v[196:199], v[132:135], v[60:63]\n"
    "v_mfma_f32_16x16x32_bf16 v[64:67], v[200:203], v[128:131], v[64:67]\n"
    "ds_read_b128 v[220:223], %3 offset:26624\n"
    "v_mfma_f32_16x16x32_bf16 v[68:71], v[200:203], v[132:135], v[68:71]\n"
    "s_waitcnt lgkmcnt(2)\n"
    "v_mfma_f32_16x16x32_bf16 v[72:75], v[204:207], v[128:131], v[72:75]\n"
    "ds_read_b128 v[224:227], %3 offset:27648\n"
    "v_mfma_f32_16x16x32_bf16 v[76:79], v[204:207], v[132:135], v[76:79]\n"
    "v_mfma_f32_16x16x32_bf16 v[80:83], v[208:211], v[128:131], v[80:83]\n"
    "ds_read_b128 v[228:231], %3 offset:28672\n"
    "v_mfma_f32_16x16x32_bf16 v[84:87], v[208:211], v[132:135], v[84:87]\n"
    "v_mfma_f32_16x16x32_bf16 v[88:91], v[212:215], v[128:131], v[88:91]\n"
    "ds_read_b128 v[232:235], %3 offset:29696\n"
    "v_mfma_f32_16x16x32_bf16 v[92:95], v[212:215], v[132:135], v[92:95]\n"
    "s_waitcnt vmcnt(8) lgkmcnt(3)\n"
    "v_mfma_f32_16x16x32_bf16 v[56:59], v[216:219], v[136:139], v[56:59]\n"
    "ds_read_b128 v[196:199], %3 offset:30720\n"
    "v_mfma_f32_16x16x32_bf16 v[60:63], v[216:219], v[140:143], v[60:63]\n"
    "v_mfma_f32_16x16x32_bf16 v[64:67], v[220:223], v[136:139], v[64:67]\n"
    "ds_read_b128 v[200:203], %3 offset:31744\n"
    "v_mfma_f32_16x16x32_bf16 v[68:71], v[220:223], v[140:143], v[68:71]\n"
    "s_waitcnt lgkmcnt(2)\n"
    "v_mfma_f32_16x16x32_bf16 v[72:75], v[224:227], v[136:139], v[72:75]\n"
    "ds_read_b128 v[204:207], %3 offset:32768\n"
    "v_mfma_f32_16x16x32_bf16 v[76:79], v[224:227], v[140:143], v[76:79]\n"
    "v_mfma_f32_16x16x32_bf16 v[80:83], v[228:231], v[136:139], v[80:83]\n"
    "ds_read_b128 v[208:211], %3 offset:33792\n"
    "v_mfma_f32_16x16x32_bf16 v[84:87], v[228:231], v[140:143], v[84:87]\n"
    "v_mfma_f32_16x16x32_bf16 v[88:91], v[232:235], v[136:139], v[88:91]\n"
    "ds_read_b128 v[212:215], %3 offset:34816\n"
    "v_mfma_f32_16x16x32_bf16 v[92:95], v[232:235], v[140:143], v[92:95]\n"
    "s_waitcnt vmcnt(4) lgkmcnt(3)\n"
    "v_mfma_f32_16x16x32_bf16 v[56:59], v[196:199], v[144:147], v[56:59]\n"
    "ds_read_b128 v[216:219], %3 offset:35840\n"
    "v_mfma_f32_16x16x32_bf16 v[60:63], v[196:199], v[148:151], v[60:63]\n"
    "v_mfma_f32_16x16x32_bf16 v[64:67], v[200:203], v[144:147], v[64:67]\n"
    "ds_read_b128 v[220:223], %3 offset:36864\n"
    "v_mfma_f32_16x16x32_bf16 v[68:71], v[200:203], v[148:151], v[68:71]\n"
    "s_waitcnt lgkmcnt(2)\n"
    "v_mfma_f32_16x16x32_bf16 v[72:75], v[204:207], v[144:147], v[72:75]\n"
    "ds_read_b128 v[224:227], %3 offset:37888\n"
    "v_mfma_f32_16x16x32_bf16 v[76:79], v[204:207], v[148:151], v[76:79]\n"
    "v_mfma_f32_16x16x32_bf16 v[80:83], v[208:211], v[144:147], v[80:83]\n"
    "ds_read_b128 v[228:231], %3 offset:38912\n"
    "v_mfma_f32_16x16x32_bf16 v[84:87], v[208:211], v[148:151], v[84:87]\n"
    "v_mfma_f32_16x16x32_bf16 v[88:91], v[212:215], v[144:147], v[88:91]\n"
    "ds_read_b128 v[232:235], %3 offset:39936\n"
    "v_mfma_f32_16x16x32_bf16 v[92:95], v[212:215], v[148:151], v[92:95]\n"
    "s_waitcnt vmcnt(0) lgkmcnt(3)\n"
    "v_mfma_f32_16x16x32_bf16 v[56:59], v[216:219], v[152:155], v[56:59]\n"
    "v_mfma_f32_16x16x32_bf16 v[60:63], v[216:219], v[156:159], v[60:63]\n"
    "v_mfma_f32_16x16x32_bf16 v[64:67], v[220:223], v[152:155], v[64:67]\n"
    "v_mfma_f32_16x16x32_bf16 v[68:71], v[220:223], v[156:159], v[68:71]\n"
    "s_waitcnt lgkmcnt(0)\n"
    "v_mfma_f32_16x16x32_bf16 v[72:75], v[224:227], v[152:155], v[72:75]\n"
    "v_mfma_f32_16x16x32_bf16 v[76:79], v[224:227], v[156:159], v[76:79]\n"
    "v_mfma_f32_16x16x32_bf16 v[80:83], v[228:231], v[152:155], v[80:83]\n"
    "v_mfma_f32_16x16x32_bf16 v[84:87], v[228:231], v[156:159], v[84:87]\n"
    "v_mfma_f32_16x16x32_bf16 v[88:91], v[232:235], v[152:155], v[88:91]\n"
    "v_mfma_f32_16x16x32_bf16 v[92:95], v[232:235], v[156:159], v[92:95]\n"
    "s_nop 7\n"
            : "=&{v[56:71]}"(A0), "=&{v[72:87]}"(A1), "=&{v[88:95]}"(A2), "+v"(aw), "+v"(v4), "+v"(v2) : "s"(og) : "memory", "v96", "v97", "v98", "v99", "v100", "v101", "v102", "v103", "v104", "v105", "v106", "v107", "v108", "v109", "v110", "v111", "v112", "v113", "v114", "v115", "v116", "v117", "v118", "v119", "v120", "v121", "v122", "v123", "v124", "v125", "v126", "v127", "v128", "v129", "v130", "v131", "v132", "v133", "v134", "v135", "v136", "v137", "v138", "v139", "v140", "v141", "v142", "v143", "v144", "v145", "v146", "v147", "v148", "v149", "v150", "v151", "v152", "v153", "v154", "v155", "v156", "v157", "v158", "v159", "v160", "v161", "v162", "v163", "v164", "v165", "v166", "v167", "v168", "v169", "v170", "v171", "v172", "v173", "v174", "v175", "v176", "v177", "v178", "v179", "v180", "v181", "v182", "v183", "v184", "v185", "v186", "v187", "v188", "v189", "v190", "v191", "v192", "v193", "v194", "v195", "v196", "v197", "v198", "v199", "v200", "v201", "v202", "v203", "v204", "v205", "v206", "v207", "v208", "v209", "v210", "v211", "v212", "v213", "v214", "v215", "v216", "v217", "v218", "v219", "v220", "v221", "v222", "v223", "v224", "v225", "v226", "v227", "v228", "v229", "v230", "v231", "v232", "v233", "v234", "v235", "v236", "v237", "v238", "v239", "v240", "v241", "v242", "v243", "v244", "v245", "v246", "v247", "v248", "v249", "v250", "v251", "v252", "v253", "v254", "v255");
#pragma unroll
        for (int T = 0; T < 2; ++T) { lptr p = lds + K3_STG + c * 5120 + (16 * T + tau) * 160 + g * 40;
            LST8(p, u32x2{pk2(A0[4 * T], A0[4 * T + 1]), pk2(A0[4 * T + 2], A0[4 * T + 3])}); LST8(p + 8, u32x2{pk2(A0[8 + 4 * T], A0[9 + 4 * T]), pk2(A0[10 + 4 * T], A0[11 + 4 * T])});
            LST8(p + 16, u32x2{pk2(A1[4 * T], A1[4 * T + 1]), pk2(A1[4 * T + 2], A1[4 * T + 3])}); LST8(p + 24, u32x2{pk2(A1[8 + 4 * T], A1[9 + 4 * T]), pk2(A1[10 + 4 * T], A1[11 + 4 * T])});
            LST8(p + 32, u32x2{pk2(A2[4 * T], A2[4 * T + 1]), pk2(A2[4 * T + 2], A2[4 * T + 3])}); }
    } else if (blockIdx.x == 0 && w == 4) {
#pragma unroll 1
        for (int n = 0; n < (1 << 18); ++n) { unsigned long long v0, v1, v2, v3;
            asm volatile("global_load_dwordx2 %0, %4, %5 sc1\n\tglobal_load_dwordx2 %1, %4, %5 offset:64 sc1\n\tglobal_load_dwordx2 %2, %4, %5 offset:128 sc1\n\t"
                         "global_load_dwordx2 %3, %4, %5 offset:192 sc1\n\ts_waitcnt vmcnt(0)" : "=&v"(v0), "=&v"(v1), "=&v"(v2), "=&v"(v3) : "v"(CTL_FO + l * 4 * FS), "s"(ctl) : "memory");
            if (__all(v0 == tag && v1 == tag && v2 == tag && v3 == tag)) break;
            __builtin_amdgcn_s_sleep(1); }
        if (l == 0) post(ctl, 0, tag);
    }
    asm volatile("s_waitcnt lgkmcnt(0)\n\ts_barrier" ::: "memory");
    lptr sg = lds + K3_STG + w * 2560;
    char* ob = out + (size_t)(t3 * 128 + w * 16) * 2560 + (xcd >> 2) * 1280 + (slot & 7) * 160;
    const int rw = (l * 26) >> 8, ch = l - 10 * rw;
    const u32x4 o0 = LLD16(sg + l * 16), o1 = LLD16(sg + 960 + l * 16), o2 = LLD16(sg + 1920 + l * 16);
    if (l < 60) { st16out(ob, rw * 2560 + ch * 16, o0); st16out(ob + 6 * 2560, rw * 2560 + ch * 16, o1); if (l < 40) st16out(ob + 12 * 2560, rw * 2560 + ch * 16, o2); }
}

extern "C" void kernel_launch(void* const* d_in, const int* in_sizes, int n_in, void* d_out, int out_size, void* d_ws, size_t ws_size, hipStream_t stream) {
    (void)hipFuncSetAttribute((const void*)hedgehog_mha_fused, hipFuncAttributeMaxDynamicSharedMemorySize, K1_LDS);
    hedgehog_mha_fused<<<256, 512, K1_LDS, stream>>>((const char*)d_in[0], (const char*)d_in[1], (const char*)d_in[2], (const char*)d_in[4], (const char*)d_in[5], (const char*)d_in[3], (const char*)d_in[6], (char*)d_ws, (char*)d_out);
}
```
